```python
import torch

def _softmax(x, axis=-1):
    return torch.softmax(x, dim=axis)
def _gelu(x):
    return torch.nn.functional.gelu(x, approximate="tanh")
def _silu(x):
    return torch.nn.functional.silu(x)
def _sigmoid(x):
    return torch.sigmoid(x)
def _layernorm(x, axis=-1, eps=1e-5):
    m = x.mean(dim=axis, keepdim=True)
    v = x.var(dim=axis, keepdim=True, unbiased=False)
    return (x - m) / torch.sqrt(v + eps)
def _scatter_add(tgt, idx, src, axis=0):
    return tgt.clone().index_add_(axis, idx, src)
def _segment_sum(src, seg, n):
    out = torch.zeros((n,) + src.shape[1:], dtype=src.dtype)
    return out.index_add_(0, seg, src)
def _pad(x, pw):
    flat = tuple(v for pair in reversed(pw) for v in pair)
    return torch.nn.functional.pad(x, flat)

kernel_name = 'recurrent_scan__compressed_kv_cla__swiglu_ffn__decode'

def setup_inputs():

    _w = (lambda a: (lambda t: t.to(torch.bfloat16) if t.is_floating_point() else t)(torch.as_tensor(a)))
    def _A_setup():
        B, H, Sq, Sk, D, M = 4, 32, 1, 4096, 128, 512
        Dc = 4 * D
        return {
            "x": _w((lambda *s: torch.randn(*s, dtype=torch.bfloat16) * 0.05)(B, Sq, M)),
            "c_kv": _w((lambda *s: torch.randn(*s, dtype=torch.bfloat16) * 0.8)(B, Sk, Dc)),
            "w_q": _w((lambda *s: torch.randn(*s, dtype=torch.bfloat16) * 1.6)(M, H, D) * (M ** -0.5)),
            "w_uk": _w((lambda *s: torch.randn(*s, dtype=torch.bfloat16) * 1.6)(Dc, H, D) * (Dc ** -0.5)),
            "w_uv": _w((lambda *s: torch.randn(*s, dtype=torch.bfloat16) * 0.8)(Dc, H, D) * (Dc ** -0.5)),
            "w_o": _w((lambda *s: torch.randn(*s, dtype=torch.bfloat16) * 0.8)(H, D, M) * ((H * D) ** -0.5)),
        }
    _A_in = _A_setup()

    def _B_setup():
        B, S, D, F = 4, 1, 512, 1792
        return {
            "x": _w((lambda *s: torch.randn(*s, dtype=torch.bfloat16) * 0.05)(B, S, D)),
            "w_gate": _w((lambda *s: torch.randn(*s, dtype=torch.bfloat16) * 0.2)(D, F)),
            "w_up": _w((lambda *s: torch.randn(*s, dtype=torch.bfloat16) * 0.2)(D, F)),
            "w_down": _w((lambda *s: torch.randn(*s, dtype=torch.bfloat16) * 0.2)(F, D)),
        }
    _B_in = _B_setup()

    return {
        "A_x": _A_in["x"],
        "A_c_kv": _A_in["c_kv"],
        "A_w_q": _A_in["w_q"],
        "A_w_uk": _A_in["w_uk"],
        "A_w_uv": _A_in["w_uv"],
        "A_w_o": _A_in["w_o"],
        "B_x": _B_in["x"],
        "B_w_gate": _B_in["w_gate"],
        "B_w_up": _B_in["w_up"],
        "B_w_down": _B_in["w_down"],
    }

def reference(A_x, A_c_kv, A_w_q, A_w_uk, A_w_uv, A_w_o, B_x, B_w_gate, B_w_up, B_w_down):
    def _A_ref(x, c_kv, w_q, w_uk, w_uv, w_o):
        B, Sq, M = x.shape[0], x.shape[1], x.shape[2]
        Sk = c_kv.shape[1]
        H, D = w_q.shape[1], w_q.shape[2]
        q = torch.einsum("bqm,mhd->bhqd", x, w_q)
        k = torch.einsum("bkc,chd->bhkd", c_kv, w_uk)
        v = torch.einsum("bkc,chd->bhkd", c_kv, w_uv)
        scores = torch.einsum("bhqd,bhkd->bhqk", q, k) * (D ** -0.5)
        off = Sk - Sq
        qi = torch.arange(Sq)[:, None]
        kj = torch.arange(Sk)[None, :]
        cmask = kj <= (qi + off)
        scores = torch.where(cmask[None, None], scores, -1e9)
        p = _softmax(scores, axis=-1)
        ctx = torch.einsum("bhqk,bhkd->bhqd", p, v)
        return torch.einsum("bhqd,hdm->bqm", ctx, w_o)

    def _B_ref(x, w_gate, w_up, w_down):
        g = _silu(x @ w_gate)
        u = x @ w_up
        return (g * u) @ w_down

    x = A_x
    xn = _layernorm(x, axis=-1)
    s = _A_ref(xn, A_c_kv, A_w_q, A_w_uk, A_w_uv, A_w_o)
    g = _silu(_B_ref(xn, B_w_gate, B_w_up, B_w_down))
    return x + s * g
```

```cpp
#include <hip/hip_runtime.h>
#include <hip/hip_bf16.h>
#include <stdint.h>

typedef unsigned short u16;
__device__ __forceinline__ float bf2f(u16 b) { return __uint_as_float(((unsigned)b) << 16); }
__device__ __forceinline__ u16 f2bf(float f) { unsigned u = __float_as_uint(f); u += 0x7FFFu + ((u >> 16) & 1u); return (u16)(u >> 16); }

constexpr int B = 4, H = 32, SK = 4096, D = 128, M = 512, DC = 512, F = 1792;

constexpr size_t OFF_XN = 0, OFF_Q = OFF_XN + 2048, OFF_H = OFF_Q + 4 * 4096, OFF_QA = OFF_H + 4 * 1792, OFF_Y = OFF_QA + 4 * 32 * 512,
                 OFF_PART = OFF_Y + 2048, OFF_ML = OFF_PART + (size_t)256 * 32 * 512, OFF_CTX = OFF_ML + 256 * 32 * 2;

__device__ void do_ln(const u16* x, float* xn  ) {
  int w = threadIdx.x >> 6, l = threadIdx.x & 63;
  float v[8]; float s = 0;
  for (int j = 0; j < 8; ++j) { v[j] = bf2f(x[w * 512 + l * 8 + j]); s += v[j]; }
  for (int m = 32; m >= 1; m >>= 1) s += __shfl_xor(s, m);
  float mean = s / 512.f; float q = 0;
  for (int j = 0; j < 8; ++j) { float d = v[j] - mean; q += d * d; }
  for (int m = 32; m >= 1; m >>= 1) q += __shfl_xor(q, m);
  float rs = 1.f / sqrtf(q / 512.f + 1e-5f);
  for (int j = 0; j < 8; ++j) xn[w * 512 + l * 8 + j] = bf2f(f2bf((v[j] - mean) * rs));
  __syncthreads();
}

__global__ __launch_bounds__(256) void k1(const u16* x, const u16* wq, const u16* wg, const u16* wu, float* ws) {
  __shared__ float xn[4 * 512];
  do_ln(x, xn);
  int n = blockIdx.x * 256 + threadIdx.x;
  if (n < 4096) {
    float a[4] = {0, 0, 0, 0};
    for (int k = 0; k < 512; ++k) { float w = bf2f(wq[(size_t)k * 4096 + n]); for (int b = 0; b < 4; ++b) a[b] += xn[b * 512 + k] * w; }
    for (int b = 0; b < 4; ++b) ws[OFF_Q + b * 4096 + n] = a[b];
  } else {
    n -= 4096;
    if (n < F) {
      float g[4] = {0, 0, 0, 0}, u[4] = {0, 0, 0, 0};
      for (int k = 0; k < 512; ++k) { float w1 = bf2f(wg[(size_t)k * F + n]), w2 = bf2f(wu[(size_t)k * F + n]); for (int b = 0; b < 4; ++b) { g[b] += xn[b * 512 + k] * w1; u[b] += xn[b * 512 + k] * w2; } }
      for (int b = 0; b < 4; ++b) ws[OFF_H + b * F + n] = g[b] / (1.f + __expf(-g[b])) * u[b];
    }
  }
}
__global__ __launch_bounds__(256) void k2(const u16* wuk, const u16* wd, float* ws) {
  int n = blockIdx.x * 256 + threadIdx.x;
  if (n < 32 * 512) {
    int h = n >> 9, c = n & 511;
    float a[4] = {0, 0, 0, 0};
    for (int d = 0; d < 128; ++d) { float w = bf2f(wuk[((size_t)c * 32 + h) * 128 + d]); for (int b = 0; b < 4; ++b) a[b] += ws[OFF_Q + b * 4096 + h * 128 + d] * w; }
    for (int b = 0; b < 4; ++b) ws[OFF_QA + ((size_t)b * 32 + h) * 512 + c] = a[b] * 0.08838834764831845f;
  } else {
    n -= 32 * 512;
    if (n < 512) {
      float a[4] = {0, 0, 0, 0};
      for (int f = 0; f < F; ++f) { float w = bf2f(wd[(size_t)f * 512 + n]); for (int b = 0; b < 4; ++b) a[b] += ws[OFF_H + b * F + f] * w; }
      for (int b = 0; b < 4; ++b) ws[OFF_Y + b * 512 + n] = a[b] / (1.f + __expf(-a[b]));
    }
  }
}
__global__ __launch_bounds__(256) void k3(const u16* ckv, float* ws) {
  __shared__ float s[32][64];
  __shared__ u16 kv[64][512 + 8];
  int b = blockIdx.x >> 6, ch = blockIdx.x & 63;
  const u16* src = ckv + ((size_t)b * SK + ch * 64) * 512;
  for (int i = threadIdx.x; i < 64 * 512; i += 256) kv[i >> 9][i & 511] = src[i];
  __syncthreads();
  for (int i = threadIdx.x; i < 32 * 64; i += 256) {
    int h = i >> 6, k = i & 63; float a = 0;
    const float* qa = ws + OFF_QA + ((size_t)b * 32 + h) * 512;
    for (int c = 0; c < 512; ++c) a += qa[c] * bf2f(kv[k][c]);
    s[h][k] = a;
  }
  __syncthreads();
  if (threadIdx.x < 32) {
    int h = threadIdx.x; float m = -1e30f; for (int k = 0; k < 64; ++k) m = fmaxf(m, s[h][k]);
    float l = 0; for (int k = 0; k < 64; ++k) { float p = __expf(s[h][k] - m); s[h][k] = p; l += p; }
    ws[OFF_ML + ((size_t)blockIdx.x * 32 + h) * 2] = m; ws[OFF_ML + ((size_t)blockIdx.x * 32 + h) * 2 + 1] = l;
  }
  __syncthreads();
  for (int i = threadIdx.x; i < 32 * 512; i += 256) {
    int h = i >> 9, c = i & 511; float a = 0;
    for (int k = 0; k < 64; ++k) a += s[h][k] * bf2f(kv[k][c]);
    ws[OFF_PART + ((size_t)blockIdx.x * 32 + h) * 512 + c] = a;
  }
}
__global__ __launch_bounds__(256) void k4(const u16* wuv, float* ws) {
  __shared__ float cc[512]; __shared__ float sc[64];
  int b = blockIdx.x >> 5, h = blockIdx.x & 31;
  if (threadIdx.x < 64) {
    float m = ws[OFF_ML + ((size_t)(b * 64 + threadIdx.x) * 32 + h) * 2], l = ws[OFF_ML + ((size_t)(b * 64 + threadIdx.x) * 32 + h) * 2 + 1];
    float mm = m; for (int o = 32; o >= 1; o >>= 1) mm = fmaxf(mm, __shfl_xor(mm, o));
    float e = __expf(m - mm); float ll = l * e; for (int o = 32; o >= 1; o >>= 1) ll += __shfl_xor(ll, o);
    sc[threadIdx.x] = e / ll;
  }
  __syncthreads();
  for (int c = threadIdx.x; c < 512; c += 256) { float a = 0; for (int i = 0; i < 64; ++i) a += sc[i] * ws[OFF_PART + ((size_t)(b * 64 + i) * 32 + h) * 512 + c]; cc[c] = a; }
  __syncthreads();
  if (threadIdx.x < 128) { int d = threadIdx.x; float a = 0; for (int c = 0; c < 512; ++c) a += cc[c] * bf2f(wuv[((size_t)c * 32 + h) * 128 + d]); ws[OFF_CTX + b * 4096 + h * 128 + d] = a; }
}
__global__ __launch_bounds__(256) void k5(const u16* x, const u16* wo, const float* ws, u16* out) {
  int m = blockIdx.x * 64 + (threadIdx.x & 63); int part = threadIdx.x >> 6;
  __shared__ float red[4][4][64];
  float a[4] = {0, 0, 0, 0};
  for (int k = part * 1024; k < part * 1024 + 1024; ++k) { float w = bf2f(wo[(size_t)k * 512 + m]); for (int b = 0; b < 4; ++b) a[b] += ws[OFF_CTX + b * 4096 + k] * w; }
  for (int b = 0; b < 4; ++b) red[part][b][threadIdx.x & 63] = a[b];
  __syncthreads();
  int b = part; float s = red[0][b][threadIdx.x & 63] + red[1][b][threadIdx.x & 63] + red[2][b][threadIdx.x & 63] + red[3][b][threadIdx.x & 63];
  out[b * 512 + m] = f2bf(bf2f(x[b * 512 + m]) + s * ws[OFF_Y + b * 512 + m]);
}

extern "C" void kernel_launch(void* const* d_in, const int* in_sizes, int n_in, void* d_out, int out_size, void* d_ws, size_t ws_size, hipStream_t stream) {
  const u16* x = (const u16*)d_in[0]; const u16* ckv = (const u16*)d_in[1]; const u16* wq = (const u16*)d_in[2]; const u16* wuk = (const u16*)d_in[3];
  const u16* wuv = (const u16*)d_in[4]; const u16* wo = (const u16*)d_in[5]; const u16* wg = (const u16*)d_in[7]; const u16* wu = (const u16*)d_in[8]; const u16* wd = (const u16*)d_in[9];
  float* ws = (float*)d_ws;
  k1<<<16 + 7, 256, 0, stream>>>(x, wq, wg, wu, ws);
  k2<<<64 + 2, 256, 0, stream>>>(wuk, wd, ws);
  k3<<<256, 256, 0, stream>>>(ckv, ws);
  k4<<<128, 256, 0, stream>>>(wuv, ws);
  k5<<<8, 256, 0, stream>>>(x, wo, ws, (u16*)d_out);
}
```

```cpp
#include <hip/hip_runtime.h>
#include <stdint.h>

typedef unsigned char u8;
typedef unsigned short u16;
typedef unsigned int u32;
typedef unsigned long long u64;
typedef u32 v4u __attribute__((ext_vector_type(4)));
typedef u32 v2u __attribute__((ext_vector_type(2)));
typedef float f32x4 __attribute__((ext_vector_type(4)));
typedef short bf16x8 __attribute__((ext_vector_type(8)));
typedef short s16x4 __attribute__((ext_vector_type(4)));
#define GAS __attribute__((address_space(1)))
#define LAS __attribute__((address_space(3)))
#define DI __device__ __forceinline__

constexpr int NWG = 256, NTHR = 512;
constexpr u32 SENT = 0xFFFFFFFFu;
constexpr int LDS_BYTES = 163840;

constexpr u32 WS_EPOCH = 0;
constexpr u32 WS_QF = 1024;
constexpr u32 WS_QP = 4096;
constexpr u32 WS_FP = WS_QP + 128 * 512 * 8;
constexpr u32 WS_QA = WS_FP + 28 * 4 * 512 * 8;
constexpr u32 WS_PART = WS_QA + 4 * 32768;
constexpr u32 WS_ST = WS_PART + 128 * 32768;
constexpr u32 WS_OUTP = WS_ST + 128 * 1024;
constexpr u32 WS_YP = WS_OUTP + 128 * 8192;
constexpr u32 WS_END = WS_YP + 128 * 7168;
constexpr u32 WS_STAMP = 200u << 20;

constexpr int AUX_SC1 = 16;
constexpr u32 SPIN_CAP = 1u << 17;

#define STAMP(k) do { } while (0)

struct Params {
  const u8* x; const u8* ckv; const u8* wq; const u8* wuk; const u8* wuv; const u8* wo;
  const u8* wg; const u8* wu; const u8* wd; u8* out; u8* ws;
};

DI float bf_lo(u32 v) { return __uint_as_float(v << 16); }
DI float bf_hi(u32 v) { return __uint_as_float(v & 0xFFFF0000u); }
DI u32 cvtpk(float lo, float hi) {
  typedef __bf16 bf2 __attribute__((ext_vector_type(2)));
  typedef float f2 __attribute__((ext_vector_type(2)));
  f2 f = {lo, hi};
  bf2 r = __builtin_convertvector(f, bf2);
  return __builtin_bit_cast(u32, r);
}
DI u32 nosent(u32 v) { return v < 0xFFFFFFFEu ? v : 0xFFFFFFFEu; }
DI v4u ldnt(const u8* p) { return __builtin_nontemporal_load((const GAS v4u*)p); }
DI f32x4 mfma(bf16x8 a, bf16x8 b, f32x4 c) { return __builtin_amdgcn_mfma_f32_16x16x32_bf16(a, b, c, 0, 0, 0); }
DI bf16x8 as_frag(v4u v) { return __builtin_bit_cast(bf16x8, v); }
DI u32 swz16(u32 k) { const u32 k2 = (k >> 2) & 1u, k3 = (k >> 3) & 1u; return k2 | ((k & 3u) << 1) | ((k2 ^ k3) << 3); }
DI void wg_barrier() { asm volatile("s_waitcnt lgkmcnt(0)\n\ts_barrier" ::: "memory"); }
DI void drain_vm() { asm volatile("s_waitcnt vmcnt(0)" ::: "memory"); }
DI float xrow16_max(float x) {
  auto s = __builtin_amdgcn_permlane16_swap(__float_as_uint(x), __float_as_uint(x), false, false);
  x = fmaxf(__uint_as_float(s[0]), __uint_as_float(s[1]));
  auto t = __builtin_amdgcn_permlane32_swap(__float_as_uint(x), __float_as_uint(x), false, false);
  return fmaxf(__uint_as_float(t[0]), __uint_as_float(t[1]));
}
DI float xrow16_sum(float x) {
  auto s = __builtin_amdgcn_permlane16_swap(__float_as_uint(x), __float_as_uint(x), false, false);
  x = __uint_as_float(s[0]) + __uint_as_float(s[1]);
  auto t = __builtin_amdgcn_permlane32_swap(__float_as_uint(x), __float_as_uint(x), false, false);
  return __uint_as_float(t[0]) + __uint_as_float(t[1]);
}
template <int CTRL> DI float dpp(float x) {
  return __builtin_bit_cast(float, __builtin_amdgcn_mov_dpp(__builtin_bit_cast(int, x), CTRL, 0xf, 0xf, true));
}
DI float row16_sum(float v) {
  v += dpp<0xB1>(v);
  v += dpp<0x4E>(v);
  v += dpp<0x124>(v);
  v += dpp<0x128>(v);
  return v;
}
DI float wave_sum(float v) { return xrow16_sum(row16_sum(v)); }

DI bf16x8 tr_frag256(LAS u8* img, int row_base  , int rowmask, int nt, int lane) {
  const int g = lane >> 4, i = lane & 15, q = i >> 2, pp = i & 3;
  const u32 r0 = (u32)(row_base + ((8 * g + q) & rowmask)), r1 = (u32)(row_base + ((8 * g + 4 + q) & rowmask));
  const u32 ch = 2 * nt + (pp >> 1), o8 = 8 * (pp & 1);
  const s16x4 a = __builtin_amdgcn_ds_read_tr16_b64_v4i16((LAS s16x4*)(img + r0 * 256 + ((ch ^ swz16(r0 & 15)) << 4) + o8));
  const s16x4 b = __builtin_amdgcn_ds_read_tr16_b64_v4i16((LAS s16x4*)(img + r1 * 256 + ((ch ^ swz16(r1 & 15)) << 4) + o8));
  return __builtin_shufflevector(a, b, 0, 1, 2, 3, 4, 5, 6, 7);
}
DI void img256_write(LAS u8* img, int r0, int lane, v4u v) {
  const u32 row = (u32)(r0 + (lane >> 4));
  *(LAS v4u*)(img + row * 256 + (((u32)(lane & 15) ^ swz16(row & 15)) << 4)) = v;
}
DI void team_bar(LAS u32* cnt, u32 target, int lane) {
  asm volatile("s_waitcnt lgkmcnt(0)" ::: "memory");
  if (lane == 0) __hip_atomic_fetch_add(cnt, 1u, __ATOMIC_RELAXED, __HIP_MEMORY_SCOPE_WORKGROUP);
  while (__hip_atomic_load(cnt, __ATOMIC_RELAXED, __HIP_MEMORY_SCOPE_WORKGROUP) < target) { }
  asm volatile("" ::: "memory");
}

#define LD128(off) __builtin_amdgcn_raw_buffer_load_b128(rs, (off), 0, AUX_SC1)
#define LD64(off) __builtin_amdgcn_raw_buffer_load_b64(rs, (off), 0, AUX_SC1)
#define LD32(off) __builtin_amdgcn_raw_buffer_load_b32(rs, (off), 0, AUX_SC1)
#define ST128(v, off) __builtin_amdgcn_raw_buffer_store_b128((v), rs, (off), 0, AUX_SC1)
#define ST64(v, off) __builtin_amdgcn_raw_buffer_store_b64((v), rs, (off), 0, AUX_SC1)
#define ST32(v, off) __builtin_amdgcn_raw_buffer_store_b32((v), rs, (off), 0, AUX_SC1)

constexpr int WL_XN = 0;
constexpr int WL_Q = 4224;
constexpr int WL_CC = 5376;
constexpr int WL_CX = 6528;
constexpr int WL_HB = 7680;
constexpr int WL_CNT = 8064;
constexpr int WL_IMG0 = 8192;
constexpr int WL_IMG1 = WL_IMG0 + 32768;
constexpr int WL_SCR = WL_IMG1 + 32768;
constexpr int WL_SCRF = WL_SCR + 65536;
static_assert(WL_SCRF + 16384 <= LDS_BYTES, "lds");
constexpr int AL_KV = 0;
constexpr int AL_Q = 131072;
constexpr int AL_MX = AL_Q;
constexpr int AL_LS = AL_Q + 1024;
constexpr int AL_P = AL_Q + 2048;
constexpr int AL_RED = AL_Q + 10240;

__global__ __launch_bounds__(NTHR, 2) void mla_ffn_kernel(Params p) {
  extern __shared__ __attribute__((aligned(16))) u8 smem[];
  LAS u8* L = (LAS u8*)smem;
  const int tid = threadIdx.x, lane = tid & 63;
  const int w = __builtin_amdgcn_readfirstlane(tid >> 6);
  const int bid = blockIdx.x, xcd = bid & 7, rr = bid >> 3;
  const __amdgpu_buffer_rsrc_t rs = __builtin_amdgcn_make_buffer_rsrc((void*)p.ws, 0, (int)WS_END, 0x00020000);
  const int g = lane >> 4, l15 = lane & 15;
  STAMP(0);
  const u32 tag = LD32(WS_EPOCH) + 1u;

  if (xcd < 4) {
    const int h = xcd * 8 + (rr >> 2), j = rr & 3, wi = h * 4 + j;
    v4u xv = {0, 0, 0, 0};
    if (w < 4) xv = *(const GAS v4u*)(p.x + w * 1024 + lane * 16);
    v4u wqv[4];
#pragma unroll
    for (int i = 0; i < 4; ++i)
      wqv[i] = ldnt(p.wq + ((size_t)(128 * j + 16 * w + 4 * i + g) * 4096 + h * 128) * 2 + l15 * 16);
    v4u wuk[4];
#pragma unroll
    for (int s = 0; s < 4; ++s)
      wuk[s] = ldnt(p.wuk + ((size_t)(128 * j + 16 * w + l15) * 4096 + h * 128 + 32 * s + 8 * g) * 2);
    __builtin_amdgcn_sched_barrier(0);
    {
      const v4u sv = {SENT, SENT, SENT, SENT};
#pragma unroll
      for (int i = 0; i < 4; ++i) ST128(sv, WS_PART + wi * 32768 + (i * 512 + tid) * 16);
      if (tid < 64) ST128(sv, WS_ST + wi * 1024 + tid * 16);
    }
    if (tid < 4) *(LAS u32*)(L + WL_CNT + tid * 4) = 0;
    if (w < 4) {
      float v[8] = {bf_lo(xv.x), bf_hi(xv.x), bf_lo(xv.y), bf_hi(xv.y), bf_lo(xv.z), bf_hi(xv.z), bf_lo(xv.w), bf_hi(xv.w)};
      float s = 0.f;
#pragma unroll
      for (int i = 0; i < 8; ++i) s += v[i];
      const float mean = wave_sum(s) * (1.f / 512.f);
      float q2 = 0.f;
#pragma unroll
      for (int i = 0; i < 8; ++i) { v[i] -= mean; q2 += v[i] * v[i]; }
      const float rstd = 1.f / sqrtf(wave_sum(q2) * (1.f / 512.f) + 1e-5f);
      v4u o;
      o.x = cvtpk(v[0] * rstd, v[1] * rstd); o.y = cvtpk(v[2] * rstd, v[3] * rstd);
      o.z = cvtpk(v[4] * rstd, v[5] * rstd); o.w = cvtpk(v[6] * rstd, v[7] * rstd);
      *(LAS v4u*)(L + WL_XN + w * 1040 + lane * 16) = o;
    }
#pragma unroll
    for (int i = 0; i < 4; ++i) img256_write(L + WL_IMG0, 16 * w + 4 * i, lane, wqv[i]);
    wg_barrier();
    STAMP(1);
    {
      f32x4 acc = {0, 0, 0, 0};
#pragma unroll
      for (int s = 0; s < 4; ++s) {
        const bf16x8 a = tr_frag256(L + WL_IMG0, 32 * s, 31, w, lane);
        const v4u b = *(LAS v4u*)(L + WL_XN + (l15 & 3) * 1040 + (128 * j + 32 * s + 8 * g) * 2);
        acc = mfma(a, as_frag(b), acc);
      }
      if (l15 < 4) {
        const u32 off = WS_QP + ((wi * 4 + l15) * 128 + 16 * w + 4 * g) * 8;
        const v4u a = {__float_as_uint(acc[0]), tag, __float_as_uint(acc[1]), tag};
        const v4u b = {__float_as_uint(acc[2]), tag, __float_as_uint(acc[3]), tag};
        ST128(a, off); ST128(b, off + 16);
      }
    }
    STAMP(2);
    {
      const int b = tid >> 7, d = tid & 127;
      const u32 off = WS_QP + ((h * 16 + b) * 128 + d) * 8;
      float sum;
      for (u32 spin = 0;; ++spin) {
        v2u v[4];
#pragma unroll
        for (int s = 0; s < 4; ++s) v[s] = LD64(off + s * 4096);
        const bool ok = (v[0].y == tag) & (v[1].y == tag) & (v[2].y == tag) & (v[3].y == tag);
        sum = (__uint_as_float(v[0].x) + __uint_as_float(v[1].x)) + (__uint_as_float(v[2].x) + __uint_as_float(v[3].x));
        if (__all(ok) || spin > SPIN_CAP) break;
        __builtin_amdgcn_s_sleep(1);
      }
      *(LAS u16*)(L + WL_Q + b * 272 + d * 2) = (u16)cvtpk(sum, 0.f);
    }
    wg_barrier();
    STAMP(3);
    {
      f32x4 acc = {0, 0, 0, 0};
#pragma unroll
      for (int s = 0; s < 4; ++s) {
        const v4u b = *(LAS v4u*)(L + WL_Q + (l15 & 3) * 272 + (32 * s + 8 * g) * 2);
        acc = mfma(as_frag(wuk[s]), as_frag(b), acc);
      }
      constexpr float SC = 0.08838834764831845f * 1.4426950408889634f;
      if (l15 < 4) {
        const int s = 4 * j + (w >> 1), gg = 2 * (w & 1) + (g >> 1);
        const u32 off = WS_QA + l15 * 32768 + (((h >> 4) * 16 + s) * 64 + gg * 16 + (h & 15)) * 16 + 8 * (g & 1);
        const v2u o = {cvtpk(acc[0] * SC, acc[1] * SC), cvtpk(acc[2] * SC, acc[3] * SC)};
        ST64(o, off);
      }
    }
    drain_vm();
    wg_barrier();
    if (tid == 0) ST32(tag, WS_QF + wi * 4);
    STAMP(4);

    if (w < 4) {
      LAS u32* tb = (LAS u32*)(L + WL_CNT);
      {
        v4u t[8];
#pragma unroll
        for (int i = 0; i < 8; ++i)
          t[i] = ldnt(p.wuv + ((size_t)(128 * j + 32 * w + 4 * i + g) * 4096 + h * 128) * 2 + l15 * 16);
#pragma unroll
        for (int i = 0; i < 8; ++i) img256_write(L + WL_IMG0, 32 * w + 4 * i, lane, t[i]);
      }
      team_bar(tb, 4, lane);
      bf16x8 fuv[2][4];
#pragma unroll
      for (int n = 0; n < 2; ++n)
#pragma unroll
        for (int s = 0; s < 4; ++s) fuv[n][s] = tr_frag256(L + WL_IMG0, 32 * s, 31, 2 * w + n, lane);
      bf16x8 fo[4][8];
      {
        LAS u8* scr = L + WL_SCR + w * 16384;
#pragma unroll
        for (int ss = 0; ss < 4; ++ss) {
          const int s = (ss + j) & 3;
          v4u t[8];
#pragma unroll
          for (int i = 0; i < 8; ++i)
            t[i] = *(const GAS v4u*)(p.wo + ((size_t)(h * 128 + 32 * s + 4 * i + g) * 512 + 128 * w) * 2 + l15 * 16);
#pragma unroll
          for (int i = 0; i < 8; ++i) img256_write(scr + (ss & 1) * 8192, 4 * i, lane, t[i]);
#pragma unroll
          for (int n = 0; n < 8; ++n) fo[ss][n] = tr_frag256(scr + (ss & 1) * 8192, 0, 31, n, lane);
        }
      }
      STAMP(5);
      const int b = w;
      u32 asc[8];
      float invL;
      {
        float m[8], lsum[8];
        for (u32 spin = 0;; ++spin) {
          bool ok = true;
#pragma unroll
          for (int s = 0; s < 8; ++s) {
            const v2u v = LD64(WS_ST + wi * 1024 + (b * 32 + 4 * s + g) * 8);
            ok &= (v.x != SENT) & (v.y != SENT);
            m[s] = __uint_as_float(v.x); lsum[s] = __uint_as_float(v.y);
          }
          if (__all(ok) || spin > SPIN_CAP) break;
          __builtin_amdgcn_s_sleep(2);
        }
        float M = m[0];
#pragma unroll
        for (int s = 1; s < 8; ++s) M = fmaxf(M, m[s]);
        M = xrow16_max(M);
        float Ls = 0.f;
#pragma unroll
        for (int s = 0; s < 8; ++s) {
          const u32 e = cvtpk(__builtin_amdgcn_exp2f(m[s] - M), 0.f) & 0xFFFFu;
          asc[s] = e;
          Ls += bf_lo(e) * lsum[s];
        }
        Ls = xrow16_sum(Ls);
        invL = 1.f / Ls;
      }
      team_bar(tb, 8, lane);
      if (lane == 0 && w == 0) __hip_atomic_store((LAS u32*)(L + WL_CNT + 8), 1u, __ATOMIC_RELAXED, __HIP_MEMORY_SCOPE_WORKGROUP);
      STAMP(6);
      {
        v4u v[8];
        for (u32 spin = 0;; ++spin) {
          bool ok = true;
#pragma unroll
          for (int s = 0; s < 8; ++s) {
            v[s] = LD128(WS_PART + wi * 32768 + ((b * 32 + 4 * s + g) * 128) * 2 + l15 * 16);
            ok &= (v[s].x != SENT) & (v[s].y != SENT) & (v[s].z != SENT) & (v[s].w != SENT);
          }
          if (__all(ok) || spin > SPIN_CAP) break;
          __builtin_amdgcn_s_sleep(1);
        }
        STAMP(7);
        const int jj = l15;
        const u32 sh = (jj & 1) * 16;
        f32x4 acc = {0, 0, 0, 0};
#pragma unroll
        for (int s = 0; s < 8; ++s) {
          const u32 val = asc[s] << sh;
          v4u a;
          a.x = (jj >> 1) == 0 ? val : 0u; a.y = (jj >> 1) == 1 ? val : 0u;
          a.z = (jj >> 1) == 2 ? val : 0u; a.w = (jj >> 1) == 3 ? val : 0u;
          acc = mfma(as_frag(a), as_frag(v[s]), acc);
        }
        if (g < 2) {
          const v2u o = {cvtpk(acc[0] * invL, acc[1] * invL), cvtpk(acc[2] * invL, acc[3] * invL)};
          *(LAS v2u*)(L + WL_CC + b * 272 + (32 * (l15 >> 2) + 16 * g + 4 * (l15 & 3)) * 2) = o;
        }
      }
      team_bar(tb, 12, lane);
      {
        v4u bb[4];
#pragma unroll
        for (int s = 0; s < 4; ++s) bb[s] = *(LAS v4u*)(L + WL_CC + (l15 & 3) * 272 + (32 * s + 8 * g) * 2);
#pragma unroll
        for (int n = 0; n < 2; ++n) {
          f32x4 acc = {0, 0, 0, 0};
#pragma unroll
          for (int s = 0; s < 4; ++s) acc = mfma(fuv[n][s], as_frag(bb[s]), acc);
          if (l15 < 4) {
            const v2u o = {cvtpk(acc[0], acc[1]), cvtpk(acc[2], acc[3])};
            *(LAS v2u*)(L + WL_CX + l15 * 272 + (16 * (2 * w + n) + 4 * g) * 2) = o;
          }
        }
      }
      team_bar(tb, 16, lane);
      {
        v4u bb[4];
#pragma unroll
        for (int ss = 0; ss < 4; ++ss)
          bb[ss] = *(LAS v4u*)(L + WL_CX + (l15 & 3) * 272 + (32 * ((ss + j) & 3) + 8 * g) * 2);
#pragma unroll
        for (int n = 0; n < 8; ++n) {
          f32x4 acc = {0, 0, 0, 0};
#pragma unroll
          for (int ss = 0; ss < 4; ++ss) acc = mfma(fo[ss][n], as_frag(bb[ss]), acc);
          if (l15 < 4) {
            const v4u o = {nosent(__float_as_uint(acc[0])), nosent(__float_as_uint(acc[1])), nosent(__float_as_uint(acc[2])), nosent(__float_as_uint(acc[3]))};
            ST128(o, WS_OUTP + (((l15 * 32 + 8 * w + n) * 128 + wi) * 16 + 4 * g) * 4);
          }
        }
      }
      STAMP(8);
    } else if (h < 28) {
      const int w4 = w - 4;
      LAS u32* fb_ = (LAS u32*)(L + WL_CNT + 4);
      {
        v4u t[8];
        const u8* base = (l15 < 8) ? p.wg : p.wu;
#pragma unroll
        for (int i = 0; i < 8; ++i)
          t[i] = ldnt(base + ((size_t)(128 * j + 32 * w4 + 4 * i + g) * 1792 + h * 64) * 2 + (l15 & 7) * 16);
        v4u d[4];
#pragma unroll
        for (int i = 0; i < 4; ++i)
          d[i] = ldnt(p.wd + ((size_t)(h * 64 + 16 * j + 4 * i + g) * 512 + 128 * w4) * 2 + l15 * 16);
#pragma unroll
        for (int i = 0; i < 8; ++i) img256_write(L + WL_IMG1, 32 * w4 + 4 * i, lane, t[i]);
#pragma unroll
        for (int i = 0; i < 4; ++i) img256_write(L + WL_SCRF + w4 * 4096, 4 * i, lane, d[i]);
      }
      team_bar(fb_, 4, lane);
      {
        f32x4 ag = {0, 0, 0, 0}, au = {0, 0, 0, 0};
#pragma unroll
        for (int s = 0; s < 4; ++s) {
          const v4u b = *(LAS v4u*)(L + WL_XN + (l15 & 3) * 1040 + (128 * j + 32 * s + 8 * g) * 2);
          ag = mfma(tr_frag256(L + WL_IMG1, 32 * s, 31, w4, lane), as_frag(b), ag);
          au = mfma(tr_frag256(L + WL_IMG1, 32 * s, 31, 4 + w4, lane), as_frag(b), au);
        }
        if (l15 < 4) {
          const u32 off = WS_FP + ((((h * 4 + j) * 2 + 0) * 4 + l15) * 64 + 16 * w4 + 4 * g) * 8;
          const v4u a0 = {__float_as_uint(ag[0]), tag, __float_as_uint(ag[1]), tag};
          const v4u a1 = {__float_as_uint(ag[2]), tag, __float_as_uint(ag[3]), tag};
          const v4u b0 = {__float_as_uint(au[0]), tag, __float_as_uint(au[1]), tag};
          const v4u b1 = {__float_as_uint(au[2]), tag, __float_as_uint(au[3]), tag};
          ST128(a0, off); ST128(a1, off + 16); ST128(b0, off + 2048); ST128(b1, off + 2048 + 16);
        }
      }
      bf16x8 fd[8];
#pragma unroll
      for (int n = 0; n < 8; ++n) fd[n] = tr_frag256(L + WL_SCRF + w4 * 4096, 0, 15, n, lane);
      if (w4 == 0) {
        const int b = g, f = l15;
        const u32 off = WS_FP + ((h * 8) * 4 + b) * 512 + (16 * j + f) * 8;
        float gs, us;
        for (u32 spin = 0;; ++spin) {
          v2u vg[4], vu[4];
#pragma unroll
          for (int s = 0; s < 4; ++s) { vg[s] = LD64(off + s * 4096); vu[s] = LD64(off + s * 4096 + 2048); }
          bool ok = true;
#pragma unroll
          for (int s = 0; s < 4; ++s) ok &= (vg[s].y == tag) & (vu[s].y == tag);
          gs = (__uint_as_float(vg[0].x) + __uint_as_float(vg[1].x)) + (__uint_as_float(vg[2].x) + __uint_as_float(vg[3].x));
          us = (__uint_as_float(vu[0].x) + __uint_as_float(vu[1].x)) + (__uint_as_float(vu[2].x) + __uint_as_float(vu[3].x));
          if (__all(ok) || spin > SPIN_CAP) break;
          __builtin_amdgcn_s_sleep(1);
        }
        const float hv = gs / (1.f + __expf(-gs)) * us;
        *(LAS u16*)(L + WL_HB + b * 80 + f * 2) = (u16)cvtpk(hv, 0.f);
        *(LAS u16*)(L + WL_HB + b * 80 + 32 + f * 2) = 0;
      }
      team_bar(fb_, 8, lane);
      f32x4 y[8];
      {
        const v4u bb = *(LAS v4u*)(L + WL_HB + (l15 & 3) * 80 + (8 * g) * 2);
#pragma unroll
        for (int n = 0; n < 8; ++n) { f32x4 z = {0, 0, 0, 0}; y[n] = mfma(fd[n], as_frag(bb), z); }
      }
      for (u32 spin = 0; __hip_atomic_load((LAS u32*)(L + WL_CNT + 8), __ATOMIC_RELAXED, __HIP_MEMORY_SCOPE_WORKGROUP) == 0 && spin < (SPIN_CAP << 6); ++spin) __builtin_amdgcn_s_sleep(1);
      asm volatile("" ::: "memory");
      if (l15 < 4) {
        const int src = h * 4 + j;
#pragma unroll
        for (int n = 0; n < 8; ++n) {
          const v4u o = {nosent(__float_as_uint(y[n][0])), nosent(__float_as_uint(y[n][1])), nosent(__float_as_uint(y[n][2])), nosent(__float_as_uint(y[n][3]))};
          ST128(o, WS_YP + (((l15 * 32 + 8 * w4 + n) * 112 + src) * 16 + 4 * g) * 4);
        }
      }
    }
  } else {
    const int b = xcd - 4, ch = rr, a = b * 32 + ch;
    if (w < 4) {
      constexpr int WIN = 10;
      const u8* src = p.ckv + ((size_t)(b * 4096 + ch * 128) * 512) * 2 + lane * 16;
      v4u r[WIN];
#pragma unroll
      for (int i = 0; i < WIN; ++i) r[i] = ldnt(src + (4 * i + w) * 1024);
      __builtin_amdgcn_sched_barrier(0);
#pragma unroll
      for (int i = 0; i < 32; ++i) {
        const v4u v = r[i % WIN];
        const u32 key = 4 * i + w;
        *(LAS v4u*)(L + AL_KV + key * 1024 + (((u32)(lane & 48) | ((u32)l15 ^ swz16(key & 15))) << 4)) = v;
        if (i + WIN < 32) r[i % WIN] = ldnt(src + (4 * (i + WIN) + w) * 1024);
        __builtin_amdgcn_sched_barrier(0);
      }
      STAMP(1);
    } else if (w == 4) {
      const v4u sv = {SENT, SENT, SENT, SENT};
#pragma unroll
      for (int i = 0; i < 8; ++i) ST128(sv, WS_OUTP + a * 8192 + i * 1024 + lane * 16);
#pragma unroll
      for (int i = 0; i < 7; ++i) ST128(sv, WS_YP + a * 7168 + i * 1024 + lane * 16);
      for (u32 spin = 0;; ++spin) {
        const v2u f = LD64(WS_QF + lane * 8);
        if (__all((f.x == tag) & (f.y == tag)) || spin > SPIN_CAP) break;
        __builtin_amdgcn_s_sleep(4);
      }
      __builtin_amdgcn_fence(__ATOMIC_ACQUIRE, "wavefront");
#pragma unroll
      for (int q = 0; q < 2; ++q) {
        v4u t[16];
#pragma unroll
        for (int i = 0; i < 16; ++i) t[i] = LD128(WS_QA + b * 32768 + (q * 16 + i) * 1024 + lane * 16);
#pragma unroll
        for (int i = 0; i < 16; ++i) *(LAS v4u*)(L + AL_Q + (q * 16 + i) * 1024 + lane * 16) = t[i];
      }
      drain_vm();
    }
    wg_barrier();
    STAMP(4);
    f32x4 s0 = {0, 0, 0, 0}, s1 = {0, 0, 0, 0};
    {
      const u32 key = 16 * w + l15;
      const u32 sw = swz16(l15);
#pragma unroll
      for (int s = 0; s < 16; ++s) {
        const u32 chn = 4 * s + g;
        const v4u av = *(LAS v4u*)(L + AL_KV + key * 1024 + (((chn & 48) | ((chn & 15) ^ sw)) << 4));
        const v4u b0 = *(LAS v4u*)(L + AL_Q + (s * 64 + lane) * 16);
        const v4u b1 = *(LAS v4u*)(L + AL_Q + ((16 + s) * 64 + lane) * 16);
        s0 = mfma(as_frag(av), as_frag(b0), s0);
        s1 = mfma(as_frag(av), as_frag(b1), s1);
      }
    }
    float m0 = xrow16_max(fmaxf(fmaxf(s0[0], s0[1]), fmaxf(s0[2], s0[3])));
    float m1 = xrow16_max(fmaxf(fmaxf(s1[0], s1[1]), fmaxf(s1[2], s1[3])));
    wg_barrier();
    if (g == 0) {
      *(LAS float*)(L + AL_MX + (l15 * 8 + w) * 4) = m0;
      *(LAS float*)(L + AL_MX + ((16 + l15) * 8 + w) * 4) = m1;
    }
    wg_barrier();
    STAMP(5);
    float M0, M1;
    {
      const f32x4 a0 = *(LAS f32x4*)(L + AL_MX + l15 * 32), a1 = *(LAS f32x4*)(L + AL_MX + l15 * 32 + 16);
      const f32x4 c0 = *(LAS f32x4*)(L + AL_MX + (16 + l15) * 32), c1 = *(LAS f32x4*)(L + AL_MX + (16 + l15) * 32 + 16);
      M0 = fmaxf(fmaxf(fmaxf(a0[0], a0[1]), fmaxf(a0[2], a0[3])), fmaxf(fmaxf(a1[0], a1[1]), fmaxf(a1[2], a1[3])));
      M1 = fmaxf(fmaxf(fmaxf(c0[0], c0[1]), fmaxf(c0[2], c0[3])), fmaxf(fmaxf(c1[0], c1[1]), fmaxf(c1[2], c1[3])));
    }
    {
      const u32 p00 = cvtpk(__builtin_amdgcn_exp2f(s0[0] - M0), __builtin_amdgcn_exp2f(s0[1] - M0));
      const u32 p01 = cvtpk(__builtin_amdgcn_exp2f(s0[2] - M0), __builtin_amdgcn_exp2f(s0[3] - M0));
      const u32 p10 = cvtpk(__builtin_amdgcn_exp2f(s1[0] - M1), __builtin_amdgcn_exp2f(s1[1] - M1));
      const u32 p11 = cvtpk(__builtin_amdgcn_exp2f(s1[2] - M1), __builtin_amdgcn_exp2f(s1[3] - M1));
      const float l0 = xrow16_sum((bf_lo(p00) + bf_hi(p00)) + (bf_lo(p01) + bf_hi(p01)));
      const float l1 = xrow16_sum((bf_lo(p10) + bf_hi(p10)) + (bf_lo(p11) + bf_hi(p11)));
      const int ks = w >> 1, gg = 2 * (w & 1) + (g >> 1);
      const v2u o0 = {p00, p01}, o1 = {p10, p11};
      *(LAS v2u*)(L + AL_P + ((0 * 4 + ks) * 64 + gg * 16 + l15) * 16 + 8 * (g & 1)) = o0;
      *(LAS v2u*)(L + AL_P + ((1 * 4 + ks) * 64 + gg * 16 + l15) * 16 + 8 * (g & 1)) = o1;
      if (g == 0) {
        *(LAS float*)(L + AL_LS + (l15 * 8 + w) * 4) = l0;
        *(LAS float*)(L + AL_LS + ((16 + l15) * 8 + w) * 4) = l1;
      }
    }
    wg_barrier();
    STAMP(6);
    if (w == 7) {
      const int hd = lane & 31;
      const f32x4 a0 = *(LAS f32x4*)(L + AL_MX + hd * 32), a1 = *(LAS f32x4*)(L + AL_MX + hd * 32 + 16);
      const f32x4 c0 = *(LAS f32x4*)(L + AL_LS + hd * 32), c1 = *(LAS f32x4*)(L + AL_LS + hd * 32 + 16);
      const float M = fmaxf(fmaxf(fmaxf(a0[0], a0[1]), fmaxf(a0[2], a0[3])), fmaxf(fmaxf(a1[0], a1[1]), fmaxf(a1[2], a1[3])));
      const float Ls = ((c0[0] + c0[1]) + (c0[2] + c0[3])) + ((c1[0] + c1[1]) + (c1[2] + c1[3]));
      const v2u o = {nosent(__float_as_uint(M)), nosent(__float_as_uint(Ls))};
      const int j0 = (lane >> 5) * 2;
      ST64(o, WS_ST + (hd * 4 + j0) * 1024 + a * 8);
      ST64(o, WS_ST + (hd * 4 + j0 + 1) * 1024 + a * 8);
    }
    {
      v4u pb[2][4];
#pragma unroll
      for (int ht = 0; ht < 2; ++ht)
#pragma unroll
        for (int ks = 0; ks < 4; ++ks) pb[ht][ks] = *(LAS v4u*)(L + AL_P + ((ht * 4 + ks) * 64 + lane) * 16);
      const int q = l15 >> 2, pp = l15 & 3;
      f32x4 acc[4][2];
#pragma unroll
      for (int ct = 0; ct < 4; ++ct) {
        acc[ct][0] = f32x4{0, 0, 0, 0}; acc[ct][1] = f32x4{0, 0, 0, 0};
        const u32 chn = (u32)(8 * w + 2 * ct + (pp >> 1));
#pragma unroll
        for (int ks = 0; ks < 4; ++ks) {
          const u32 k0 = 32 * ks + 8 * g + q, k1 = k0 + 4;
          const s16x4 x0 = __builtin_amdgcn_ds_read_tr16_b64_v4i16((LAS s16x4*)(L + AL_KV + k0 * 1024 + (((chn & 48) | ((chn & 15) ^ swz16(k0 & 15))) << 4) + 8 * (pp & 1)));
          const s16x4 x1 = __builtin_amdgcn_ds_read_tr16_b64_v4i16((LAS s16x4*)(L + AL_KV + k1 * 1024 + (((chn & 48) | ((chn & 15) ^ swz16(k1 & 15))) << 4) + 8 * (pp & 1)));
          const bf16x8 af = __builtin_shufflevector(x0, x1, 0, 1, 2, 3, 4, 5, 6, 7);
          acc[ct][0] = mfma(af, as_frag(pb[0][ks]), acc[ct][0]);
          acc[ct][1] = mfma(af, as_frag(pb[1][ks]), acc[ct][1]);
        }
      }
#pragma unroll
      for (int ht = 0; ht < 2; ++ht)
#pragma unroll
        for (int T = 0; T < 2; ++T) {
          v4u o;
          o.x = nosent(cvtpk(acc[2 * T][ht][0], acc[2 * T][ht][1])); o.y = nosent(cvtpk(acc[2 * T][ht][2], acc[2 * T][ht][3]));
          o.z = nosent(cvtpk(acc[2 * T + 1][ht][0], acc[2 * T + 1][ht][1])); o.w = nosent(cvtpk(acc[2 * T + 1][ht][2], acc[2 * T + 1][ht][3]));
          ST128(o, WS_PART + ((16 * ht + l15) * 4 + (w >> 1)) * 32768 + (a * 128 + 64 * (w & 1) + 32 * T + 8 * g) * 2);
        }
    }
    STAMP(7);
    {
      const int src = tid >> 2, qd = tid & 3;
      v4u vy = {0, 0, 0, 0}, vs;
      for (u32 spin = 0;; ++spin) {
        bool ok = true;
        if (src < 112) { vy = LD128(WS_YP + a * 7168 + tid * 16); ok &= (vy.x != SENT) & (vy.y != SENT) & (vy.z != SENT) & (vy.w != SENT); }
        vs = LD128(WS_OUTP + a * 8192 + tid * 16);
        ok &= (vs.x != SENT) & (vs.y != SENT) & (vs.z != SENT) & (vs.w != SENT);
        if (__all(ok) || spin > SPIN_CAP) break;
        __builtin_amdgcn_s_sleep(2);
      }
      STAMP(8);
      float r[8] = {__uint_as_float(vs.x), __uint_as_float(vs.y), __uint_as_float(vs.z), __uint_as_float(vs.w),
                    __uint_as_float(vy.x), __uint_as_float(vy.y), __uint_as_float(vy.z), __uint_as_float(vy.w)};
#pragma unroll
      for (int i = 0; i < 8; ++i) { float v = r[i]; v += dpp<0x124>(v); v += dpp<0x128>(v); r[i] = xrow16_sum(v); }
      if (lane < 4) {
        *(LAS f32x4*)(L + AL_RED + (w * 2 + 0) * 64 + qd * 16) = f32x4{r[0], r[1], r[2], r[3]};
        *(LAS f32x4*)(L + AL_RED + (w * 2 + 1) * 64 + qd * 16) = f32x4{r[4], r[5], r[6], r[7]};
      }
      wg_barrier();
      if (tid < 16) {
        float s = 0.f, y = 0.f;
#pragma unroll
        for (int ww = 0; ww < 8; ++ww) {
          s += *(LAS float*)(L + AL_RED + (ww * 2 + 0) * 64 + tid * 4);
          y += *(LAS float*)(L + AL_RED + (ww * 2 + 1) * 64 + tid * 4);
        }
        const int o = b * 512 + ch * 16 + tid;
        const float xv = bf_lo((u32) * (const GAS u16*)(p.x + o * 2));
        const float gt = y / (1.f + __expf(-y));
        *(GAS u16*)(p.out + o * 2) = (u16)cvtpk(xv + s * gt, 0.f);
      }
      if (a == 0 && tid == 0) ST32(tag, WS_EPOCH);
      STAMP(9);
    }
  }
}

extern "C" void kernel_launch(void* const* d_in, const int* in_sizes, int n_in, void* d_out, int out_size, void* d_ws, size_t ws_size,
                              hipStream_t stream) {
  Params p;
  p.x = (const u8*)d_in[0]; p.ckv = (const u8*)d_in[1]; p.wq = (const u8*)d_in[2]; p.wuk = (const u8*)d_in[3];
  p.wuv = (const u8*)d_in[4]; p.wo = (const u8*)d_in[5]; p.wg = (const u8*)d_in[7]; p.wu = (const u8*)d_in[8]; p.wd = (const u8*)d_in[9];
  p.out = (u8*)d_out; p.ws = (u8*)d_ws;
  static bool attr = false;
  if (!attr) { (void)hipFuncSetAttribute((const void*)mla_ffn_kernel, hipFuncAttributeMaxDynamicSharedMemorySize, LDS_BYTES); attr = true; }
  hipLaunchKernelGGL(mla_ffn_kernel, dim3(NWG), dim3(NTHR), LDS_BYTES, stream, p);
}
```

```cpp
#include <hip/hip_runtime.h>
#include <stdint.h>

typedef unsigned char u8;
typedef unsigned short u16;
typedef unsigned int u32;
typedef unsigned long long u64;
typedef u32 v4u __attribute__((ext_vector_type(4)));
typedef u32 v2u __attribute__((ext_vector_type(2)));
typedef float f32x4 __attribute__((ext_vector_type(4)));
typedef short bf16x8 __attribute__((ext_vector_type(8)));
typedef short s16x4 __attribute__((ext_vector_type(4)));
#define GAS __attribute__((address_space(1)))
#define LAS __attribute__((address_space(3)))
#define DI __device__ __forceinline__

constexpr int NWG = 256, NTHR = 512;
#define CODE_KB 19
constexpr u32 SENT = 0xFFFFFFFFu;
constexpr int LDS_BYTES = 163840;

constexpr u32 WS_EPOCH = 0;
constexpr u32 WS_QF = 1024;
constexpr u32 WS_QP = 16384;
constexpr u32 WS_FP = WS_QP + 128 * 512 * 8;
constexpr u32 WS_QA = WS_FP + 28 * 4 * 512 * 8;
constexpr u32 WS_PART = WS_QA + 4 * 32768;
constexpr u32 WS_ST = WS_PART + 128 * 32768;
constexpr u32 WS_OUTP = WS_ST + 128 * 1024;
constexpr u32 WS_YP = WS_OUTP + 128 * 8192;
constexpr u32 WS_END = WS_YP + 128 * 7168;
constexpr u32 WS_STAMP = 200u << 20;

constexpr int AUX_SC1 = 16;
constexpr u32 SPIN_CAP = 1u << 17;

#define STAMP(k) do { } while (0)

struct Params {
  const u8* x; const u8* ckv; const u8* wq; const u8* wuk; const u8* wuv; const u8* wo;
  const u8* wg; const u8* wu; const u8* wd; u8* out; u8* ws;
};

DI float bf_lo(u32 v) { return __uint_as_float(v << 16); }
DI float bf_hi(u32 v) { return __uint_as_float(v & 0xFFFF0000u); }
DI u32 cvtpk(float lo, float hi) {
  typedef __bf16 bf2 __attribute__((ext_vector_type(2)));
  typedef float f2 __attribute__((ext_vector_type(2)));
  f2 f = {lo, hi};
  bf2 r = __builtin_convertvector(f, bf2);
  return __builtin_bit_cast(u32, r);
}
DI u32 nosent(u32 v) { return v < 0xFFFFFFFEu ? v : 0xFFFFFFFEu; }
DI v4u ldnt(const u8* p) { return __builtin_nontemporal_load((const GAS v4u*)p); }
DI f32x4 mfma(bf16x8 a, bf16x8 b, f32x4 c) { return __builtin_amdgcn_mfma_f32_16x16x32_bf16(a, b, c, 0, 0, 0); }
DI bf16x8 as_frag(v4u v) { return __builtin_bit_cast(bf16x8, v); }
DI u32 swz16(u32 k) { const u32 k2 = (k >> 2) & 1u, k3 = (k >> 3) & 1u; return k2 | ((k & 3u) << 1) | ((k2 ^ k3) << 3); }
DI void wg_barrier() { asm volatile("s_waitcnt lgkmcnt(0)\n\ts_barrier" ::: "memory"); }
DI void drain_vm() { asm volatile("s_waitcnt vmcnt(0)" ::: "memory"); }
DI float xrow16_max(float x) {
  auto s = __builtin_amdgcn_permlane16_swap(__float_as_uint(x), __float_as_uint(x), false, false);
  x = fmaxf(__uint_as_float(s[0]), __uint_as_float(s[1]));
  auto t = __builtin_amdgcn_permlane32_swap(__float_as_uint(x), __float_as_uint(x), false, false);
  return fmaxf(__uint_as_float(t[0]), __uint_as_float(t[1]));
}
DI float xrow16_sum(float x) {
  auto s = __builtin_amdgcn_permlane16_swap(__float_as_uint(x), __float_as_uint(x), false, false);
  x = __uint_as_float(s[0]) + __uint_as_float(s[1]);
  auto t = __builtin_amdgcn_permlane32_swap(__float_as_uint(x), __float_as_uint(x), false, false);
  return __uint_as_float(t[0]) + __uint_as_float(t[1]);
}
template <int CTRL> DI float dpp(float x) {
  return __builtin_bit_cast(float, __builtin_amdgcn_mov_dpp(__builtin_bit_cast(int, x), CTRL, 0xf, 0xf, true));
}
DI float row16_sum(float v) {
  v += dpp<0xB1>(v);
  v += dpp<0x4E>(v);
  v += dpp<0x124>(v);
  v += dpp<0x128>(v);
  return v;
}
DI float wave_sum(float v) { return xrow16_sum(row16_sum(v)); }

DI bf16x8 tr_frag256(LAS u8* img, int row_base  , int rowmask, int nt, int lane) {
  const int g = lane >> 4, i = lane & 15, q = i >> 2, pp = i & 3;
  const u32 r0 = (u32)(row_base + ((8 * g + q) & rowmask)), r1 = (u32)(row_base + ((8 * g + 4 + q) & rowmask));
  const u32 ch = 2 * nt + (pp >> 1), o8 = 8 * (pp & 1);
  const s16x4 a = __builtin_amdgcn_ds_read_tr16_b64_v4i16((LAS s16x4*)(img + r0 * 256 + ((ch ^ swz16(r0 & 15)) << 4) + o8));
  const s16x4 b = __builtin_amdgcn_ds_read_tr16_b64_v4i16((LAS s16x4*)(img + r1 * 256 + ((ch ^ swz16(r1 & 15)) << 4) + o8));
  return __builtin_shufflevector(a, b, 0, 1, 2, 3, 4, 5, 6, 7);
}
DI void img256_write(LAS u8* img, int r0, int lane, v4u v) {
  const u32 row = (u32)(r0 + (lane >> 4));
  *(LAS v4u*)(img + row * 256 + (((u32)(lane & 15) ^ swz16(row & 15)) << 4)) = v;
}
DI void team_bar(LAS u32* cnt, u32 target, int lane) {
  asm volatile("s_waitcnt lgkmcnt(0)" ::: "memory");
  if (lane == 0) __hip_atomic_fetch_add(cnt, 1u, __ATOMIC_RELAXED, __HIP_MEMORY_SCOPE_WORKGROUP);
  while (__hip_atomic_load(cnt, __ATOMIC_RELAXED, __HIP_MEMORY_SCOPE_WORKGROUP) < target) { }
  asm volatile("" ::: "memory");
}

#define LD128(off) __builtin_amdgcn_raw_buffer_load_b128(rs, (off), 0, AUX_SC1)
#define LD64(off) __builtin_amdgcn_raw_buffer_load_b64(rs, (off), 0, AUX_SC1)
#define LD32(off) __builtin_amdgcn_raw_buffer_load_b32(rs, (off), 0, AUX_SC1)
#define ST128(v, off) __builtin_amdgcn_raw_buffer_store_b128((v), rs, (off), 0, AUX_SC1)
#define ST64(v, off) __builtin_amdgcn_raw_buffer_store_b64((v), rs, (off), 0, AUX_SC1)
#define ST32(v, off) __builtin_amdgcn_raw_buffer_store_b32((v), rs, (off), 0, AUX_SC1)

constexpr int WL_XN = 0;
constexpr int WL_Q = 4224;
constexpr int WL_CC = 5376;
constexpr int WL_CX = 6528;
constexpr int WL_HB = 7680;
constexpr int WL_CNT = 8064;
constexpr int WL_IMG0 = 8192;
constexpr int WL_IMG1 = WL_IMG0 + 32768;
constexpr int WL_SCR = WL_IMG1 + 32768;
constexpr int WL_SCRF = WL_SCR + 65536;
static_assert(WL_SCRF + 16384 <= LDS_BYTES, "lds");
constexpr int AL_KV = 0;
constexpr int AL_Q = 131072;
constexpr int AL_MX = AL_Q;
constexpr int AL_LS = AL_Q + 1024;
constexpr int AL_P = AL_Q + 2048;
constexpr int AL_RED = AL_Q + 10240;

__global__ __launch_bounds__(NTHR, 2) void mla_ffn_kernel(Params p) {
  extern __shared__ __attribute__((aligned(16))) u8 smem[];
  LAS u8* L = (LAS u8*)smem;
  const int tid = threadIdx.x, lane = tid & 63;
  const int w = __builtin_amdgcn_readfirstlane(tid >> 6);
  const int bid = blockIdx.x, xcd = bid & 7, rr = bid >> 3;
  const __amdgpu_buffer_rsrc_t rs = __builtin_amdgcn_make_buffer_rsrc((void*)p.ws, 0, (int)WS_END, 0x00020000);
  const int g = lane >> 4, l15 = lane & 15;
  STAMP(0);
  u32 ep_raw = __builtin_amdgcn_raw_buffer_load_b32(rs, WS_EPOCH, 0, 0);
#define GET_TAG() u32 tag; { u32 e_ = ep_raw; asm volatile("" : "+v"(e_)); tag = e_ + 1u; }

  if (w == 7) {
    const u8* pc = (const u8*)__builtin_amdgcn_s_getpc();
    pc = (const u8*)((u64)pc & ~(u64)255);
    v4u acc = {0, 0, 0, 0};
#pragma unroll
    for (int i = 0; i < CODE_KB; ++i) { const v4u t = *(const GAS v4u*)(pc + i * 1024 + lane * 16); acc.x ^= t.x; acc.y ^= t.y; acc.z ^= t.z; acc.w ^= t.w; }
    asm volatile("" :: "v"(acc));
  }
  const bool isW = rr < 16;
  if (isW) {
    const int h = xcd * 4 + (rr >> 2), j = rr & 3, wi = h * 4 + j;
    v4u xv = {0, 0, 0, 0};
    if (w < 4) xv = *(const GAS v4u*)(p.x + w * 1024 + lane * 16);
    v4u wqv[4];
#pragma unroll
    for (int i = 0; i < 4; ++i)
      wqv[i] = ldnt(p.wq + ((size_t)(128 * j + 16 * w + 4 * i + g) * 4096 + h * 128) * 2 + l15 * 16);
    v4u wuk[4];
#pragma unroll
    for (int s = 0; s < 4; ++s)
      wuk[s] = ldnt(p.wuk + ((size_t)(128 * j + 16 * w + l15) * 4096 + h * 128 + 32 * s + 8 * g) * 2);
    __builtin_amdgcn_sched_barrier(0);
    {
      const v4u sv = {SENT, SENT, SENT, SENT};
#pragma unroll
      for (int i = 0; i < 4; ++i) ST128(sv, WS_PART + wi * 32768 + (i * 512 + tid) * 16);
      if (tid < 64) ST128(sv, WS_ST + wi * 1024 + tid * 16);
    }
    if (tid < 4) *(LAS u32*)(L + WL_CNT + tid * 4) = 0;
    if (w < 4) {
      float v[8] = {bf_lo(xv.x), bf_hi(xv.x), bf_lo(xv.y), bf_hi(xv.y), bf_lo(xv.z), bf_hi(xv.z), bf_lo(xv.w), bf_hi(xv.w)};
      float s = 0.f;
#pragma unroll
      for (int i = 0; i < 8; ++i) s += v[i];
      const float mean = wave_sum(s) * (1.f / 512.f);
      float q2 = 0.f;
#pragma unroll
      for (int i = 0; i < 8; ++i) { v[i] -= mean; q2 += v[i] * v[i]; }
      const float rstd = 1.f / sqrtf(wave_sum(q2) * (1.f / 512.f) + 1e-5f);
      v4u o;
      o.x = cvtpk(v[0] * rstd, v[1] * rstd); o.y = cvtpk(v[2] * rstd, v[3] * rstd);
      o.z = cvtpk(v[4] * rstd, v[5] * rstd); o.w = cvtpk(v[6] * rstd, v[7] * rstd);
      *(LAS v4u*)(L + WL_XN + w * 1040 + lane * 16) = o;
    }
#pragma unroll
    for (int i = 0; i < 4; ++i) img256_write(L + WL_IMG0, 16 * w + 4 * i, lane, wqv[i]);
    wg_barrier();
    STAMP(1);
    GET_TAG();
    {
      f32x4 acc = {0, 0, 0, 0};
#pragma unroll
      for (int s = 0; s < 4; ++s) {
        const bf16x8 a = tr_frag256(L + WL_IMG0, 32 * s, 31, w, lane);
        const v4u b = *(LAS v4u*)(L + WL_XN + (l15 & 3) * 1040 + (128 * j + 32 * s + 8 * g) * 2);
        acc = mfma(a, as_frag(b), acc);
      }
      if (l15 < 4) {
        const u32 off = WS_QP + ((wi * 4 + l15) * 128 + 16 * w + 4 * g) * 8;
        const v4u a = {__float_as_uint(acc[0]), tag, __float_as_uint(acc[1]), tag};
        const v4u b = {__float_as_uint(acc[2]), tag, __float_as_uint(acc[3]), tag};
        ST128(a, off); ST128(b, off + 16);
      }
    }
    STAMP(2);
    {
      const int b = tid >> 7, d = tid & 127;
      const u32 off = WS_QP + ((h * 16 + b) * 128 + d) * 8;
      float sum;
      for (u32 spin = 0;; ++spin) {
        v2u v[4];
#pragma unroll
        for (int s = 0; s < 4; ++s) v[s] = LD64(off + s * 4096);
        const bool ok = (v[0].y == tag) & (v[1].y == tag) & (v[2].y == tag) & (v[3].y == tag);
        sum = (__uint_as_float(v[0].x) + __uint_as_float(v[1].x)) + (__uint_as_float(v[2].x) + __uint_as_float(v[3].x));
        if (__all(ok) || spin > SPIN_CAP) break;
        __builtin_amdgcn_s_sleep(1);
      }
      *(LAS u16*)(L + WL_Q + b * 272 + d * 2) = (u16)cvtpk(sum, 0.f);
    }
    wg_barrier();
    STAMP(3);
    {
      f32x4 acc = {0, 0, 0, 0};
#pragma unroll
      for (int s = 0; s < 4; ++s) {
        const v4u b = *(LAS v4u*)(L + WL_Q + (l15 & 3) * 272 + (32 * s + 8 * g) * 2);
        acc = mfma(as_frag(wuk[s]), as_frag(b), acc);
      }
      constexpr float SC = 0.08838834764831845f * 1.4426950408889634f;
      if (l15 < 4) {
        const int s = 4 * j + (w >> 1), gg = 2 * (w & 1) + (g >> 1);
        const u32 off = WS_QA + l15 * 32768 + (((h >> 4) * 16 + s) * 64 + gg * 16 + (h & 15)) * 16 + 8 * (g & 1);
        const v2u o = {cvtpk(acc[0] * SC, acc[1] * SC), cvtpk(acc[2] * SC, acc[3] * SC)};
        ST64(o, off);
      }
    }
    drain_vm();
    wg_barrier();
    if (tid < 16) ST32(tag, WS_QF + tid * 512 + wi * 4);
    STAMP(4);
    if (w < 4) {
      LAS u32* tb = (LAS u32*)(L + WL_CNT);
      {
        v4u t[8];
#pragma unroll
        for (int i = 0; i < 8; ++i)
          t[i] = ldnt(p.wuv + ((size_t)(128 * j + 32 * w + 4 * i + g) * 4096 + h * 128) * 2 + l15 * 16);
#pragma unroll
        for (int i = 0; i < 8; ++i) img256_write(L + WL_IMG0, 32 * w + 4 * i, lane, t[i]);
      }
      team_bar(tb, 4, lane);
      bf16x8 fuv[2][4];
#pragma unroll
      for (int n = 0; n < 2; ++n)
#pragma unroll
        for (int s = 0; s < 4; ++s) fuv[n][s] = tr_frag256(L + WL_IMG0, 32 * s, 31, 2 * w + n, lane);
      bf16x8 fo[4][8];
      {
        LAS u8* scr = L + WL_SCR + w * 16384;
#pragma unroll
        for (int ss = 0; ss < 4; ++ss) {
          const int s = (ss + j) & 3;
          v4u t[8];
#pragma unroll
          for (int i = 0; i < 8; ++i)
            t[i] = *(const GAS v4u*)(p.wo + ((size_t)(h * 128 + 32 * s + 4 * i + g) * 512 + 128 * w) * 2 + l15 * 16);
#pragma unroll
          for (int i = 0; i < 8; ++i) img256_write(scr + (ss & 1) * 8192, 4 * i, lane, t[i]);
#pragma unroll
          for (int n = 0; n < 8; ++n) fo[ss][n] = tr_frag256(scr + (ss & 1) * 8192, 0, 31, n, lane);
        }
      }
      STAMP(5);
      const int b = w;
      u32 asc[8];
      float invL;
      {
        float m[8], lsum[8];
        for (u32 spin = 0;; ++spin) {
          bool ok = true;
#pragma unroll
          for (int s = 0; s < 8; ++s) {
            const v2u v = LD64(WS_ST + wi * 1024 + (b * 32 + 4 * s + g) * 8);
            ok &= (v.x != SENT) & (v.y != SENT);
            m[s] = __uint_as_float(v.x); lsum[s] = __uint_as_float(v.y);
          }
          if (__all(ok) || spin > SPIN_CAP) break;
          __builtin_amdgcn_s_sleep(2);
        }
        float M = m[0];
#pragma unroll
        for (int s = 1; s < 8; ++s) M = fmaxf(M, m[s]);
        M = xrow16_max(M);
        float Ls = 0.f;
#pragma unroll
        for (int s = 0; s < 8; ++s) {
          const u32 e = cvtpk(__builtin_amdgcn_exp2f(m[s] - M), 0.f) & 0xFFFFu;
          asc[s] = e;
          Ls += bf_lo(e) * lsum[s];
        }
        Ls = xrow16_sum(Ls);
        invL = 1.f / Ls;
      }
      team_bar(tb, 8, lane);
      if (lane == 0 && w == 0) __hip_atomic_store((LAS u32*)(L + WL_CNT + 8), 1u, __ATOMIC_RELAXED, __HIP_MEMORY_SCOPE_WORKGROUP);
      STAMP(6);
      {
        v4u v[8];
        for (u32 spin = 0;; ++spin) {
          bool ok = true;
#pragma unroll
          for (int s = 0; s < 8; ++s) {
            v[s] = LD128(WS_PART + wi * 32768 + ((b * 32 + 4 * s + g) * 128) * 2 + l15 * 16);
            ok &= (v[s].x != SENT) & (v[s].y != SENT) & (v[s].z != SENT) & (v[s].w != SENT);
          }
          if (__all(ok) || spin > SPIN_CAP) break;
          __builtin_amdgcn_s_sleep(1);
        }
        STAMP(7);
        const int jj = l15;
        const u32 sh = (jj & 1) * 16;
        f32x4 acc = {0, 0, 0, 0};
#pragma unroll
        for (int s = 0; s < 8; ++s) {
          const u32 val = asc[s] << sh;
          v4u a;
          a.x = (jj >> 1) == 0 ? val : 0u; a.y = (jj >> 1) == 1 ? val : 0u;
          a.z = (jj >> 1) == 2 ? val : 0u; a.w = (jj >> 1) == 3 ? val : 0u;
          acc = mfma(as_frag(a), as_frag(v[s]), acc);
        }
        if (g < 2) {
          const v2u o = {cvtpk(acc[0] * invL, acc[1] * invL), cvtpk(acc[2] * invL, acc[3] * invL)};
          *(LAS v2u*)(L + WL_CC + b * 272 + (32 * (l15 >> 2) + 16 * g + 4 * (l15 & 3)) * 2) = o;
        }
      }
      team_bar(tb, 12, lane);
      {
        v4u bb[4];
#pragma unroll
        for (int s = 0; s < 4; ++s) bb[s] = *(LAS v4u*)(L + WL_CC + (l15 & 3) * 272 + (32 * s + 8 * g) * 2);
#pragma unroll
        for (int n = 0; n < 2; ++n) {
          f32x4 acc = {0, 0, 0, 0};
#pragma unroll
          for (int s = 0; s < 4; ++s) acc = mfma(fuv[n][s], as_frag(bb[s]), acc);
          if (l15 < 4) {
            const v2u o = {cvtpk(acc[0], acc[1]), cvtpk(acc[2], acc[3])};
            *(LAS v2u*)(L + WL_CX + l15 * 272 + (16 * (2 * w + n) + 4 * g) * 2) = o;
          }
        }
      }
      team_bar(tb, 16, lane);
      {
        v4u bb[4];
#pragma unroll
        for (int ss = 0; ss < 4; ++ss)
          bb[ss] = *(LAS v4u*)(L + WL_CX + (l15 & 3) * 272 + (32 * ((ss + j) & 3) + 8 * g) * 2);
#pragma unroll
        for (int n = 0; n < 8; ++n) {
          f32x4 acc = {0, 0, 0, 0};
#pragma unroll
          for (int ss = 0; ss < 4; ++ss) acc = mfma(fo[ss][n], as_frag(bb[ss]), acc);
          if (l15 < 4) {
            const v4u o = {nosent(__float_as_uint(acc[0])), nosent(__float_as_uint(acc[1])), nosent(__float_as_uint(acc[2])), nosent(__float_as_uint(acc[3]))};
            ST128(o, WS_OUTP + (((l15 * 32 + 8 * w + n) * 128 + wi) * 16 + 4 * g) * 4);
          }
        }
      }
      STAMP(8);
    } else if (h < 28) {
      const int w4 = w - 4;
      LAS u32* fb_ = (LAS u32*)(L + WL_CNT + 4);
      {
        v4u t[8];
        const u8* base = (l15 < 8) ? p.wg : p.wu;
#pragma unroll
        for (int i = 0; i < 8; ++i)
          t[i] = ldnt(base + ((size_t)(128 * j + 32 * w4 + 4 * i + g) * 1792 + h * 64) * 2 + (l15 & 7) * 16);
        v4u d[4];
#pragma unroll
        for (int i = 0; i < 4; ++i)
          d[i] = ldnt(p.wd + ((size_t)(h * 64 + 16 * j + 4 * i + g) * 512 + 128 * w4) * 2 + l15 * 16);
#pragma unroll
        for (int i = 0; i < 8; ++i) img256_write(L + WL_IMG1, 32 * w4 + 4 * i, lane, t[i]);
#pragma unroll
        for (int i = 0; i < 4; ++i) img256_write(L + WL_SCRF + w4 * 4096, 4 * i, lane, d[i]);
      }
      team_bar(fb_, 4, lane);
      {
        f32x4 ag = {0, 0, 0, 0}, au = {0, 0, 0, 0};
#pragma unroll
        for (int s = 0; s < 4; ++s) {
          const v4u b = *(LAS v4u*)(L + WL_XN + (l15 & 3) * 1040 + (128 * j + 32 * s + 8 * g) * 2);
          ag = mfma(tr_frag256(L + WL_IMG1, 32 * s, 31, w4, lane), as_frag(b), ag);
          au = mfma(tr_frag256(L + WL_IMG1, 32 * s, 31, 4 + w4, lane), as_frag(b), au);
        }
        if (l15 < 4) {
          const u32 off = WS_FP + ((((h * 4 + j) * 2 + 0) * 4 + l15) * 64 + 16 * w4 + 4 * g) * 8;
          const v4u a0 = {__float_as_uint(ag[0]), tag, __float_as_uint(ag[1]), tag};
          const v4u a1 = {__float_as_uint(ag[2]), tag, __float_as_uint(ag[3]), tag};
          const v4u b0 = {__float_as_uint(au[0]), tag, __float_as_uint(au[1]), tag};
          const v4u b1 = {__float_as_uint(au[2]), tag, __float_as_uint(au[3]), tag};
          ST128(a0, off); ST128(a1, off + 16); ST128(b0, off + 2048); ST128(b1, off + 2048 + 16);
        }
      }
      bf16x8 fd[8];
#pragma unroll
      for (int n = 0; n < 8; ++n) fd[n] = tr_frag256(L + WL_SCRF + w4 * 4096, 0, 15, n, lane);
      if (w4 == 0) {
        const int b = g, f = l15;
        const u32 off = WS_FP + ((h * 8) * 4 + b) * 512 + (16 * j + f) * 8;
        float gs, us;
        for (u32 spin = 0;; ++spin) {
          v2u vg[4], vu[4];
#pragma unroll
          for (int s = 0; s < 4; ++s) { vg[s] = LD64(off + s * 4096); vu[s] = LD64(off + s * 4096 + 2048); }
          bool ok = true;
#pragma unroll
          for (int s = 0; s < 4; ++s) ok &= (vg[s].y == tag) & (vu[s].y == tag);
          gs = (__uint_as_float(vg[0].x) + __uint_as_float(vg[1].x)) + (__uint_as_float(vg[2].x) + __uint_as_float(vg[3].x));
          us = (__uint_as_float(vu[0].x) + __uint_as_float(vu[1].x)) + (__uint_as_float(vu[2].x) + __uint_as_float(vu[3].x));
          if (__all(ok) || spin > SPIN_CAP) break;
          __builtin_amdgcn_s_sleep(1);
        }
        const float hv = gs / (1.f + __expf(-gs)) * us;
        *(LAS u16*)(L + WL_HB + b * 80 + f * 2) = (u16)cvtpk(hv, 0.f);
        *(LAS u16*)(L + WL_HB + b * 80 + 32 + f * 2) = 0;
      }
      team_bar(fb_, 8, lane);
      f32x4 y[8];
      {
        const v4u bb = *(LAS v4u*)(L + WL_HB + (l15 & 3) * 80 + (8 * g) * 2);
#pragma unroll
        for (int n = 0; n < 8; ++n) { f32x4 z = {0, 0, 0, 0}; y[n] = mfma(fd[n], as_frag(bb), z); }
      }
      for (u32 spin = 0; __hip_atomic_load((LAS u32*)(L + WL_CNT + 8), __ATOMIC_RELAXED, __HIP_MEMORY_SCOPE_WORKGROUP) == 0 && spin < (SPIN_CAP << 6); ++spin) __builtin_amdgcn_s_sleep(1);
      asm volatile("" ::: "memory");
      if (l15 < 4) {
        const int src = h * 4 + j;
#pragma unroll
        for (int n = 0; n < 8; ++n) {
          const v4u o = {nosent(__float_as_uint(y[n][0])), nosent(__float_as_uint(y[n][1])), nosent(__float_as_uint(y[n][2])), nosent(__float_as_uint(y[n][3]))};
          ST128(o, WS_YP + (((l15 * 32 + 8 * w4 + n) * 112 + src) * 16 + 4 * g) * 4);
        }
      }
    }
  } else {
    const int a = xcd * 16 + (rr - 16), b = a >> 5, ch = a & 31;
    if (w < 4) {
#define CKWIN 10
      constexpr int WIN = CKWIN;
      const u8* src = p.ckv + ((size_t)(b * 4096 + ch * 128) * 512) * 2 + lane * 16;
      v4u r[WIN];
#pragma unroll
      for (int i = 0; i < WIN; ++i) r[i] = ldnt(src + (4 * i + w) * 1024);
      __builtin_amdgcn_sched_barrier(0);
#pragma unroll
      for (int i = 0; i < 32; ++i) {
        const v4u v = r[i % WIN];
        const u32 key = 4 * i + w;
        *(LAS v4u*)(L + AL_KV + key * 1024 + (((u32)(lane & 48) | ((u32)l15 ^ swz16(key & 15))) << 4)) = v;
        if (i + WIN < 32) r[i % WIN] = ldnt(src + (4 * (i + WIN) + w) * 1024);
        __builtin_amdgcn_sched_barrier(0);
      }
      STAMP(1);
    } else if (w == 4) {
      const v4u sv = {SENT, SENT, SENT, SENT};
#pragma unroll
      for (int i = 0; i < 8; ++i) ST128(sv, WS_OUTP + a * 8192 + i * 1024 + lane * 16);
#pragma unroll
      for (int i = 0; i < 7; ++i) ST128(sv, WS_YP + a * 7168 + i * 1024 + lane * 16);
      GET_TAG();
      for (u32 spin = 0;; ++spin) {
        const v2u f = LD64(WS_QF + (a & 15) * 512 + lane * 8);
        if (__all((f.x == tag) & (f.y == tag)) || spin > SPIN_CAP) break;
        __builtin_amdgcn_s_sleep(4);
      }
      __builtin_amdgcn_fence(__ATOMIC_ACQUIRE, "wavefront");
#pragma unroll
      for (int q = 0; q < 2; ++q) {
        v4u t[16];
#pragma unroll
        for (int i = 0; i < 16; ++i) t[i] = LD128(WS_QA + b * 32768 + (q * 16 + i) * 1024 + lane * 16);
#pragma unroll
        for (int i = 0; i < 16; ++i) *(LAS v4u*)(L + AL_Q + (q * 16 + i) * 1024 + lane * 16) = t[i];
      }
      drain_vm();
    }
    wg_barrier();
    STAMP(4);
    f32x4 s0 = {0, 0, 0, 0}, s1 = {0, 0, 0, 0};
    {
      const u32 key = 16 * w + l15;
      const u32 sw = swz16(l15);
#pragma unroll
      for (int s = 0; s < 16; ++s) {
        const u32 chn = 4 * s + g;
        const v4u av = *(LAS v4u*)(L + AL_KV + key * 1024 + (((chn & 48) | ((chn & 15) ^ sw)) << 4));
        const v4u b0 = *(LAS v4u*)(L + AL_Q + (s * 64 + lane) * 16);
        const v4u b1 = *(LAS v4u*)(L + AL_Q + ((16 + s) * 64 + lane) * 16);
        s0 = mfma(as_frag(av), as_frag(b0), s0);
        s1 = mfma(as_frag(av), as_frag(b1), s1);
      }
    }
    float m0 = xrow16_max(fmaxf(fmaxf(s0[0], s0[1]), fmaxf(s0[2], s0[3])));
    float m1 = xrow16_max(fmaxf(fmaxf(s1[0], s1[1]), fmaxf(s1[2], s1[3])));
    wg_barrier();
    if (g == 0) {
      *(LAS float*)(L + AL_MX + (l15 * 8 + w) * 4) = m0;
      *(LAS float*)(L + AL_MX + ((16 + l15) * 8 + w) * 4) = m1;
    }
    wg_barrier();
    STAMP(5);
    float M0, M1;
    {
      const f32x4 a0 = *(LAS f32x4*)(L + AL_MX + l15 * 32), a1 = *(LAS f32x4*)(L + AL_MX + l15 * 32 + 16);
      const f32x4 c0 = *(LAS f32x4*)(L + AL_MX + (16 + l15) * 32), c1 = *(LAS f32x4*)(L + AL_MX + (16 + l15) * 32 + 16);
      M0 = fmaxf(fmaxf(fmaxf(a0[0], a0[1]), fmaxf(a0[2], a0[3])), fmaxf(fmaxf(a1[0], a1[1]), fmaxf(a1[2], a1[3])));
      M1 = fmaxf(fmaxf(fmaxf(c0[0], c0[1]), fmaxf(c0[2], c0[3])), fmaxf(fmaxf(c1[0], c1[1]), fmaxf(c1[2], c1[3])));
    }
    {
      const u32 p00 = cvtpk(__builtin_amdgcn_exp2f(s0[0] - M0), __builtin_amdgcn_exp2f(s0[1] - M0));
      const u32 p01 = cvtpk(__builtin_amdgcn_exp2f(s0[2] - M0), __builtin_amdgcn_exp2f(s0[3] - M0));
      const u32 p10 = cvtpk(__builtin_amdgcn_exp2f(s1[0] - M1), __builtin_amdgcn_exp2f(s1[1] - M1));
      const u32 p11 = cvtpk(__builtin_amdgcn_exp2f(s1[2] - M1), __builtin_amdgcn_exp2f(s1[3] - M1));
      const float l0 = xrow16_sum((bf_lo(p00) + bf_hi(p00)) + (bf_lo(p01) + bf_hi(p01)));
      const float l1 = xrow16_sum((bf_lo(p10) + bf_hi(p10)) + (bf_lo(p11) + bf_hi(p11)));
      const int ks = w >> 1, gg = 2 * (w & 1) + (g >> 1);
      const v2u o0 = {p00, p01}, o1 = {p10, p11};
      *(LAS v2u*)(L + AL_P + ((0 * 4 + ks) * 64 + gg * 16 + l15) * 16 + 8 * (g & 1)) = o0;
      *(LAS v2u*)(L + AL_P + ((1 * 4 + ks) * 64 + gg * 16 + l15) * 16 + 8 * (g & 1)) = o1;
      if (g == 0) {
        *(LAS float*)(L + AL_LS + (l15 * 8 + w) * 4) = l0;
        *(LAS float*)(L + AL_LS + ((16 + l15) * 8 + w) * 4) = l1;
      }
    }
    wg_barrier();
    STAMP(6);
    if (w == 7) {
      const int hd = lane & 31;
      const f32x4 a0 = *(LAS f32x4*)(L + AL_MX + hd * 32), a1 = *(LAS f32x4*)(L + AL_MX + hd * 32 + 16);
      const f32x4 c0 = *(LAS f32x4*)(L + AL_LS + hd * 32), c1 = *(LAS f32x4*)(L + AL_LS + hd * 32 + 16);
      const float M = fmaxf(fmaxf(fmaxf(a0[0], a0[1]), fmaxf(a0[2], a0[3])), fmaxf(fmaxf(a1[0], a1[1]), fmaxf(a1[2], a1[3])));
      const float Ls = ((c0[0] + c0[1]) + (c0[2] + c0[3])) + ((c1[0] + c1[1]) + (c1[2] + c1[3]));
      const v2u o = {nosent(__float_as_uint(M)), nosent(__float_as_uint(Ls))};
      const int j0 = (lane >> 5) * 2;
      ST64(o, WS_ST + (hd * 4 + j0) * 1024 + a * 8);
      ST64(o, WS_ST + (hd * 4 + j0 + 1) * 1024 + a * 8);
    }
    {
      v4u pb[2][4];
#pragma unroll
      for (int ht = 0; ht < 2; ++ht)
#pragma unroll
        for (int ks = 0; ks < 4; ++ks) pb[ht][ks] = *(LAS v4u*)(L + AL_P + ((ht * 4 + ks) * 64 + lane) * 16);
      const int q = l15 >> 2, pp = l15 & 3;
      f32x4 acc[4][2];
#pragma unroll
      for (int ct = 0; ct < 4; ++ct) {
        acc[ct][0] = f32x4{0, 0, 0, 0}; acc[ct][1] = f32x4{0, 0, 0, 0};
        const u32 chn = (u32)(8 * w + 2 * ct + (pp >> 1));
#pragma unroll
        for (int ks = 0; ks < 4; ++ks) {
          const u32 k0 = 32 * ks + 8 * g + q, k1 = k0 + 4;
          const s16x4 x0 = __builtin_amdgcn_ds_read_tr16_b64_v4i16((LAS s16x4*)(L + AL_KV + k0 * 1024 + (((chn & 48) | ((chn & 15) ^ swz16(k0 & 15))) << 4) + 8 * (pp & 1)));
          const s16x4 x1 = __builtin_amdgcn_ds_read_tr16_b64_v4i16((LAS s16x4*)(L + AL_KV + k1 * 1024 + (((chn & 48) | ((chn & 15) ^ swz16(k1 & 15))) << 4) + 8 * (pp & 1)));
          const bf16x8 af = __builtin_shufflevector(x0, x1, 0, 1, 2, 3, 4, 5, 6, 7);
          acc[ct][0] = mfma(af, as_frag(pb[0][ks]), acc[ct][0]);
          acc[ct][1] = mfma(af, as_frag(pb[1][ks]), acc[ct][1]);
        }
      }
#pragma unroll
      for (int ht = 0; ht < 2; ++ht)
#pragma unroll
        for (int T = 0; T < 2; ++T) {
          v4u o;
          o.x = nosent(cvtpk(acc[2 * T][ht][0], acc[2 * T][ht][1])); o.y = nosent(cvtpk(acc[2 * T][ht][2], acc[2 * T][ht][3]));
          o.z = nosent(cvtpk(acc[2 * T + 1][ht][0], acc[2 * T + 1][ht][1])); o.w = nosent(cvtpk(acc[2 * T + 1][ht][2], acc[2 * T + 1][ht][3]));
          ST128(o, WS_PART + ((16 * ht + l15) * 4 + (w >> 1)) * 32768 + (a * 128 + 64 * (w & 1) + 32 * T + 8 * g) * 2);
        }
    }
    STAMP(7);
    {
      const int src = tid >> 2, qd = tid & 3;
      v4u vy = {0, 0, 0, 0}, vs;
      for (u32 spin = 0;; ++spin) {
        bool ok = true;
        if (src < 112) { vy = LD128(WS_YP + a * 7168 + tid * 16); ok &= (vy.x != SENT) & (vy.y != SENT) & (vy.z != SENT) & (vy.w != SENT); }
        vs = LD128(WS_OUTP + a * 8192 + tid * 16);
        ok &= (vs.x != SENT) & (vs.y != SENT) & (vs.z != SENT) & (vs.w != SENT);
        if (__all(ok) || spin > SPIN_CAP) break;
        __builtin_amdgcn_s_sleep(2);
      }
      STAMP(8);
      float r[8] = {__uint_as_float(vs.x), __uint_as_float(vs.y), __uint_as_float(vs.z), __uint_as_float(vs.w),
                    __uint_as_float(vy.x), __uint_as_float(vy.y), __uint_as_float(vy.z), __uint_as_float(vy.w)};
#pragma unroll
      for (int i = 0; i < 8; ++i) { float v = r[i]; v += dpp<0x124>(v); v += dpp<0x128>(v); r[i] = xrow16_sum(v); }
      if (lane < 4) {
        *(LAS f32x4*)(L + AL_RED + (w * 2 + 0) * 64 + qd * 16) = f32x4{r[0], r[1], r[2], r[3]};
        *(LAS f32x4*)(L + AL_RED + (w * 2 + 1) * 64 + qd * 16) = f32x4{r[4], r[5], r[6], r[7]};
      }
      wg_barrier();
      if (tid < 16) {
        float s = 0.f, y = 0.f;
#pragma unroll
        for (int ww = 0; ww < 8; ++ww) {
          s += *(LAS float*)(L + AL_RED + (ww * 2 + 0) * 64 + tid * 4);
          y += *(LAS float*)(L + AL_RED + (ww * 2 + 1) * 64 + tid * 4);
        }
        const int o = b * 512 + ch * 16 + tid;
        const float xv = bf_lo((u32) * (const GAS u16*)(p.x + o * 2));
        const float gt = y / (1.f + __expf(-y));
        *(GAS u16*)(p.out + o * 2) = (u16)cvtpk(xv + s * gt, 0.f);
      }
      if (a == 0 && tid == 0) { GET_TAG(); ST32(tag, WS_EPOCH); }
      STAMP(9);
    }
  }
}

extern "C" void kernel_launch(void* const* d_in, const int* in_sizes, int n_in, void* d_out, int out_size, void* d_ws, size_t ws_size,
                              hipStream_t stream) {
  Params p;
  p.x = (const u8*)d_in[0]; p.ckv = (const u8*)d_in[1]; p.wq = (const u8*)d_in[2]; p.wuk = (const u8*)d_in[3];
  p.wuv = (const u8*)d_in[4]; p.wo = (const u8*)d_in[5]; p.wg = (const u8*)d_in[7]; p.wu = (const u8*)d_in[8]; p.wd = (const u8*)d_in[9];
  p.out = (u8*)d_out; p.ws = (u8*)d_ws;
  static bool attr = false;
  if (!attr) { (void)hipFuncSetAttribute((const void*)mla_ffn_kernel, hipFuncAttributeMaxDynamicSharedMemorySize, LDS_BYTES); attr = true; }
  hipLaunchKernelGGL(mla_ffn_kernel, dim3(NWG), dim3(NTHR), LDS_BYTES, stream, p);
}
```

```cpp
#include <hip/hip_runtime.h>
#include <stdint.h>

typedef unsigned char u8;
typedef unsigned short u16;
typedef unsigned int u32;
typedef unsigned long long u64;
typedef u32 v4u __attribute__((ext_vector_type(4)));
typedef u32 v2u __attribute__((ext_vector_type(2)));
typedef float f32x4 __attribute__((ext_vector_type(4)));
typedef short bf16x8 __attribute__((ext_vector_type(8)));
typedef short s16x4 __attribute__((ext_vector_type(4)));
#define GAS __attribute__((address_space(1)))
#define LAS __attribute__((address_space(3)))
#define DI __device__ __forceinline__

constexpr int NWG = 256, NTHR = 512;
constexpr u32 SENT = 0xFFFFFFFFu;
constexpr int LDS_BYTES = 163840;

constexpr u32 WS_EPOCH = 0;
constexpr u32 WS_QF = 1024;
constexpr u32 WS_QP = 16384;
constexpr u32 WS_FP = WS_QP + 128 * 512 * 8;
constexpr u32 WS_QA = WS_FP + 28 * 4 * 512 * 8;
constexpr u32 WS_PART = WS_QA + 4 * 32768;
constexpr u32 WS_ST = WS_PART + 128 * 32768;
constexpr u32 WS_OUTP = WS_ST + 128 * 1024;
constexpr u32 WS_YP = WS_OUTP + 128 * 8192;
constexpr u32 WS_END = WS_YP + 128 * 7168;
constexpr u32 WS_STAMP = 200u << 20;

constexpr int AUX_SC1 = 16;
constexpr u32 SPIN_CAP = 1u << 17;

#define STAMP(k) do { } while (0)

struct Params {
  const u8* x; const u8* ckv; const u8* wq; const u8* wuk; const u8* wuv; const u8* wo;
  const u8* wg; const u8* wu; const u8* wd; u8* out; u8* ws;
};

DI float bf_lo(u32 v) { return __uint_as_float(v << 16); }
DI float bf_hi(u32 v) { return __uint_as_float(v & 0xFFFF0000u); }
DI u32 cvtpk(float lo, float hi) {
  typedef __bf16 bf2 __attribute__((ext_vector_type(2)));
  typedef float f2 __attribute__((ext_vector_type(2)));
  f2 f = {lo, hi};
  bf2 r = __builtin_convertvector(f, bf2);
  return __builtin_bit_cast(u32, r);
}
DI u32 nosent(u32 v) { return v < 0xFFFFFFFEu ? v : 0xFFFFFFFEu; }
DI v4u ldnt(const u8* p) { return __builtin_nontemporal_load((const GAS v4u*)p); }
DI f32x4 mfma(bf16x8 a, bf16x8 b, f32x4 c) { return __builtin_amdgcn_mfma_f32_16x16x32_bf16(a, b, c, 0, 0, 0); }
DI bf16x8 as_frag(v4u v) { return __builtin_bit_cast(bf16x8, v); }
DI u32 swz16(u32 k) { const u32 k2 = (k >> 2) & 1u, k3 = (k >> 3) & 1u; return k2 | ((k & 3u) << 1) | ((k2 ^ k3) << 3); }
DI void wg_barrier() { asm volatile("s_waitcnt lgkmcnt(0)\n\ts_barrier" ::: "memory"); }
DI void drain_vm() { asm volatile("s_waitcnt vmcnt(0)" ::: "memory"); }
DI float xrow16_max(float x) {
  auto s = __builtin_amdgcn_permlane16_swap(__float_as_uint(x), __float_as_uint(x), false, false);
  x = fmaxf(__uint_as_float(s[0]), __uint_as_float(s[1]));
  auto t = __builtin_amdgcn_permlane32_swap(__float_as_uint(x), __float_as_uint(x), false, false);
  return fmaxf(__uint_as_float(t[0]), __uint_as_float(t[1]));
}
DI float xrow16_sum(float x) {
  auto s = __builtin_amdgcn_permlane16_swap(__float_as_uint(x), __float_as_uint(x), false, false);
  x = __uint_as_float(s[0]) + __uint_as_float(s[1]);
  auto t = __builtin_amdgcn_permlane32_swap(__float_as_uint(x), __float_as_uint(x), false, false);
  return __uint_as_float(t[0]) + __uint_as_float(t[1]);
}
template <int CTRL> DI float dpp(float x) {
  return __builtin_bit_cast(float, __builtin_amdgcn_mov_dpp(__builtin_bit_cast(int, x), CTRL, 0xf, 0xf, true));
}
DI float row16_sum(float v) {
  v += dpp<0xB1>(v);
  v += dpp<0x4E>(v);
  v += dpp<0x124>(v);
  v += dpp<0x128>(v);
  return v;
}
DI float wave_sum(float v) { return xrow16_sum(row16_sum(v)); }

DI bf16x8 tr_frag256(LAS u8* img, int row_base  , int rowmask, int nt, int lane) {
  const int g = lane >> 4, i = lane & 15, q = i >> 2, pp = i & 3;
  const u32 r0 = (u32)(row_base + ((8 * g + q) & rowmask)), r1 = (u32)(row_base + ((8 * g + 4 + q) & rowmask));
  const u32 ch = 2 * nt + (pp >> 1), o8 = 8 * (pp & 1);
  const s16x4 a = __builtin_amdgcn_ds_read_tr16_b64_v4i16((LAS s16x4*)(img + r0 * 256 + ((ch ^ swz16(r0 & 15)) << 4) + o8));
  const s16x4 b = __builtin_amdgcn_ds_read_tr16_b64_v4i16((LAS s16x4*)(img + r1 * 256 + ((ch ^ swz16(r1 & 15)) << 4) + o8));
  return __builtin_shufflevector(a, b, 0, 1, 2, 3, 4, 5, 6, 7);
}
DI void img256_write(LAS u8* img, int r0, int lane, v4u v) {
  const u32 row = (u32)(r0 + (lane >> 4));
  *(LAS v4u*)(img + row * 256 + (((u32)(lane & 15) ^ swz16(row & 15)) << 4)) = v;
}
DI void team_bar(LAS u32* cnt, u32 target, int lane) {
  asm volatile("s_waitcnt lgkmcnt(0)" ::: "memory");
  if (lane == 0) __hip_atomic_fetch_add(cnt, 1u, __ATOMIC_RELAXED, __HIP_MEMORY_SCOPE_WORKGROUP);
  while (__hip_atomic_load(cnt, __ATOMIC_RELAXED, __HIP_MEMORY_SCOPE_WORKGROUP) < target) { }
  asm volatile("" ::: "memory");
}

#define LD128(off) __builtin_amdgcn_raw_buffer_load_b128(rs, (off), 0, AUX_SC1)
#define LD64(off) __builtin_amdgcn_raw_buffer_load_b64(rs, (off), 0, AUX_SC1)
#define LD32(off) __builtin_amdgcn_raw_buffer_load_b32(rs, (off), 0, AUX_SC1)
#define ST128(v, off) __builtin_amdgcn_raw_buffer_store_b128((v), rs, (off), 0, AUX_SC1)
#define ST64(v, off) __builtin_amdgcn_raw_buffer_store_b64((v), rs, (off), 0, AUX_SC1)
#define ST32(v, off) __builtin_amdgcn_raw_buffer_store_b32((v), rs, (off), 0, AUX_SC1)

constexpr int WL_XN = 0;
constexpr int WL_Q = 4224;
constexpr int WL_CC = 5376;
constexpr int WL_CX = 6528;
constexpr int WL_HB = 7680;
constexpr int WL_CNT = 8064;
constexpr int WL_IMG0 = 8192;
constexpr int WL_IMG1 = WL_IMG0 + 32768;
constexpr int WL_SCR = WL_IMG1 + 32768;
constexpr int WL_SCRF = WL_SCR + 65536;
static_assert(WL_SCRF + 16384 <= LDS_BYTES, "lds");
constexpr int AL_KV = 0;
constexpr int AL_Q = 131072;
constexpr int AL_MX = AL_Q;
constexpr int AL_LS = AL_Q + 1024;
constexpr int AL_P = AL_Q + 2048;
constexpr int AL_RED = AL_Q + 10240;

__global__ __launch_bounds__(NTHR, 2) void mla_ffn_kernel(Params p) {
  extern __shared__ __attribute__((aligned(16))) u8 smem[];
  LAS u8* L = (LAS u8*)smem;
  const int tid = threadIdx.x, lane = tid & 63;
  const int w = __builtin_amdgcn_readfirstlane(tid >> 6);
  const int bid = blockIdx.x, xcd = bid & 7, rr = bid >> 3;
  const __amdgpu_buffer_rsrc_t rs = __builtin_amdgcn_make_buffer_rsrc((void*)p.ws, 0, (int)WS_END, 0x00020000);
  const int g = lane >> 4, l15 = lane & 15;
  STAMP(0);
  u32 ep_raw = __builtin_amdgcn_raw_buffer_load_b32(rs, WS_EPOCH, 0, 0);
#define GET_TAG() u32 tag; { u32 e_ = ep_raw; asm volatile("" : "+v"(e_)); tag = e_ + 1u; }

  const bool isW = rr < 16;
  if (isW) {
    const int h = xcd * 4 + (rr >> 2), j = rr & 3, wi = h * 4 + j;
    v4u xv = {0, 0, 0, 0};
    if (w < 4) xv = *(const GAS v4u*)(p.x + w * 1024 + lane * 16);
    v4u wqv[4];
#pragma unroll
    for (int i = 0; i < 4; ++i)
      wqv[i] = ldnt(p.wq + ((size_t)(128 * j + 16 * w + 4 * i + g) * 4096 + h * 128) * 2 + l15 * 16);
    v4u wuk[4];
#pragma unroll
    for (int s = 0; s < 4; ++s)
      wuk[s] = ldnt(p.wuk + ((size_t)(128 * j + 16 * w + l15) * 4096 + h * 128 + 32 * s + 8 * g) * 2);
    __builtin_amdgcn_sched_barrier(0);
    {
      const v4u sv = {SENT, SENT, SENT, SENT};
#pragma unroll
      for (int i = 0; i < 4; ++i) ST128(sv, WS_PART + wi * 32768 + (i * 512 + tid) * 16);
      if (tid < 64) ST128(sv, WS_ST + wi * 1024 + tid * 16);
    }
    if (tid < 4) *(LAS u32*)(L + WL_CNT + tid * 4) = 0;
    if (w < 4) {
      float v[8] = {bf_lo(xv.x), bf_hi(xv.x), bf_lo(xv.y), bf_hi(xv.y), bf_lo(xv.z), bf_hi(xv.z), bf_lo(xv.w), bf_hi(xv.w)};
      float s = 0.f;
#pragma unroll
      for (int i = 0; i < 8; ++i) s += v[i];
      const float mean = wave_sum(s) * (1.f / 512.f);
      float q2 = 0.f;
#pragma unroll
      for (int i = 0; i < 8; ++i) { v[i] -= mean; q2 += v[i] * v[i]; }
      const float rstd = 1.f / sqrtf(wave_sum(q2) * (1.f / 512.f) + 1e-5f);
      v4u o;
      o.x = cvtpk(v[0] * rstd, v[1] * rstd); o.y = cvtpk(v[2] * rstd, v[3] * rstd);
      o.z = cvtpk(v[4] * rstd, v[5] * rstd); o.w = cvtpk(v[6] * rstd, v[7] * rstd);
      *(LAS v4u*)(L + WL_XN + w * 1040 + lane * 16) = o;
    }
#pragma unroll
    for (int i = 0; i < 4; ++i) img256_write(L + WL_IMG0, 16 * w + 4 * i, lane, wqv[i]);
    wg_barrier();
    STAMP(1);
    GET_TAG();
    {
      f32x4 acc = {0, 0, 0, 0};
#pragma unroll
      for (int s = 0; s < 4; ++s) {
        const bf16x8 a = tr_frag256(L + WL_IMG0, 32 * s, 31, w, lane);
        const v4u b = *(LAS v4u*)(L + WL_XN + (l15 & 3) * 1040 + (128 * j + 32 * s + 8 * g) * 2);
        acc = mfma(a, as_frag(b), acc);
      }
      if (l15 < 4) {
        const u32 off = WS_QP + ((wi * 4 + l15) * 128 + 16 * w + 4 * g) * 8;
        const v4u a = {__float_as_uint(acc[0]), tag, __float_as_uint(acc[1]), tag};
        const v4u b = {__float_as_uint(acc[2]), tag, __float_as_uint(acc[3]), tag};
        ST128(a, off); ST128(b, off + 16);
      }
    }
    STAMP(2);
    {
      const int b = tid >> 7, d = tid & 127;
      const u32 off = WS_QP + ((h * 16 + b) * 128 + d) * 8;
      float sum;
      for (u32 spin = 0;; ++spin) {
        v2u v[4];
#pragma unroll
        for (int s = 0; s < 4; ++s) v[s] = LD64(off + s * 4096);
        const bool ok = (v[0].y == tag) & (v[1].y == tag) & (v[2].y == tag) & (v[3].y == tag);
        sum = (__uint_as_float(v[0].x) + __uint_as_float(v[1].x)) + (__uint_as_float(v[2].x) + __uint_as_float(v[3].x));
        if (__all(ok) || spin > SPIN_CAP) break;
        __builtin_amdgcn_s_sleep(1);
      }
      *(LAS u16*)(L + WL_Q + b * 272 + d * 2) = (u16)cvtpk(sum, 0.f);
    }
    wg_barrier();
    STAMP(3);
    {
      f32x4 acc = {0, 0, 0, 0};
#pragma unroll
      for (int s = 0; s < 4; ++s) {
        const v4u b = *(LAS v4u*)(L + WL_Q + (l15 & 3) * 272 + (32 * s + 8 * g) * 2);
        acc = mfma(as_frag(wuk[s]), as_frag(b), acc);
      }
      constexpr float SC = 0.08838834764831845f * 1.4426950408889634f;
      if (l15 < 4) {
        const int s = 4 * j + (w >> 1), gg = 2 * (w & 1) + (g >> 1);
        const u32 off = WS_QA + l15 * 32768 + (((h >> 4) * 16 + s) * 64 + gg * 16 + (h & 15)) * 16 + 8 * (g & 1);
        const v2u o = {cvtpk(acc[0] * SC, acc[1] * SC), cvtpk(acc[2] * SC, acc[3] * SC)};
        ST64(o, off);
      }
    }
    drain_vm();
    wg_barrier();
    if (tid < 16) ST32(tag, WS_QF + tid * 512 + wi * 4);
    STAMP(4);
    if (w < 4) {
      LAS u32* tb = (LAS u32*)(L + WL_CNT);
      {
        v4u t[8];
#pragma unroll
        for (int i = 0; i < 8; ++i)
          t[i] = ldnt(p.wuv + ((size_t)(128 * j + 32 * w + 4 * i + g) * 4096 + h * 128) * 2 + l15 * 16);
#pragma unroll
        for (int i = 0; i < 8; ++i) img256_write(L + WL_IMG0, 32 * w + 4 * i, lane, t[i]);
      }
      team_bar(tb, 4, lane);
      bf16x8 fuv[2][4];
#pragma unroll
      for (int n = 0; n < 2; ++n)
#pragma unroll
        for (int s = 0; s < 4; ++s) fuv[n][s] = tr_frag256(L + WL_IMG0, 32 * s, 31, 2 * w + n, lane);
      bf16x8 fo[4][8];
      {
        LAS u8* scr = L + WL_SCR + w * 16384;
#pragma unroll
        for (int ss = 0; ss < 4; ++ss) {
          const int s = (ss + j) & 3;
          v4u t[8];
#pragma unroll
          for (int i = 0; i < 8; ++i)
            t[i] = *(const GAS v4u*)(p.wo + ((size_t)(h * 128 + 32 * s + 4 * i + g) * 512 + 128 * w) * 2 + l15 * 16);
#pragma unroll
          for (int i = 0; i < 8; ++i) img256_write(scr + (ss & 1) * 8192, 4 * i, lane, t[i]);
#pragma unroll
          for (int n = 0; n < 8; ++n) fo[ss][n] = tr_frag256(scr + (ss & 1) * 8192, 0, 31, n, lane);
        }
      }
      STAMP(5);
      const int b = w;
      u32 asc[8];
      float invL;
      {
        float m[8], lsum[8];
        for (u32 spin = 0;; ++spin) {
          bool ok = true;
#pragma unroll
          for (int s = 0; s < 8; ++s) {
            const v2u v = LD64(WS_ST + wi * 1024 + (b * 32 + 4 * s + g) * 8);
            ok &= (v.x != SENT) & (v.y != SENT);
            m[s] = __uint_as_float(v.x); lsum[s] = __uint_as_float(v.y);
          }
          if (__all(ok) || spin > SPIN_CAP) break;
          __builtin_amdgcn_s_sleep(2);
        }
        float M = m[0];
#pragma unroll
        for (int s = 1; s < 8; ++s) M = fmaxf(M, m[s]);
        M = xrow16_max(M);
        float Ls = 0.f;
#pragma unroll
        for (int s = 0; s < 8; ++s) {
          const u32 e = cvtpk(__builtin_amdgcn_exp2f(m[s] - M), 0.f) & 0xFFFFu;
          asc[s] = e;
          Ls += bf_lo(e) * lsum[s];
        }
        Ls = xrow16_sum(Ls);
        invL = 1.f / Ls;
      }
      team_bar(tb, 8, lane);
      if (lane == 0 && w == 0) __hip_atomic_store((LAS u32*)(L + WL_CNT + 8), 1u, __ATOMIC_RELAXED, __HIP_MEMORY_SCOPE_WORKGROUP);
      STAMP(6);
      {
        v4u v[8];
        for (u32 spin = 0;; ++spin) {
          bool ok = true;
#pragma unroll
          for (int s = 0; s < 8; ++s) {
            v[s] = LD128(WS_PART + wi * 32768 + ((b * 32 + 4 * s + g) * 128) * 2 + l15 * 16);
            ok &= (v[s].x != SENT) & (v[s].y != SENT) & (v[s].z != SENT) & (v[s].w != SENT);
          }
          if (__all(ok) || spin > SPIN_CAP) break;
          __builtin_amdgcn_s_sleep(1);
        }
        STAMP(7);
        const int jj = l15;
        const u32 sh = (jj & 1) * 16;
        f32x4 acc = {0, 0, 0, 0};
#pragma unroll
        for (int s = 0; s < 8; ++s) {
          const u32 val = asc[s] << sh;
          v4u a;
          a.x = (jj >> 1) == 0 ? val : 0u; a.y = (jj >> 1) == 1 ? val : 0u;
          a.z = (jj >> 1) == 2 ? val : 0u; a.w = (jj >> 1) == 3 ? val : 0u;
          acc = mfma(as_frag(a), as_frag(v[s]), acc);
        }
        if (g < 2) {
          const v2u o = {cvtpk(acc[0] * invL, acc[1] * invL), cvtpk(acc[2] * invL, acc[3] * invL)};
          *(LAS v2u*)(L + WL_CC + b * 272 + (32 * (l15 >> 2) + 16 * g + 4 * (l15 & 3)) * 2) = o;
        }
      }
      team_bar(tb, 12, lane);
      {
        v4u bb[4];
#pragma unroll
        for (int s = 0; s < 4; ++s) bb[s] = *(LAS v4u*)(L + WL_CC + (l15 & 3) * 272 + (32 * s + 8 * g) * 2);
#pragma unroll
        for (int n = 0; n < 2; ++n) {
          f32x4 acc = {0, 0, 0, 0};
#pragma unroll
          for (int s = 0; s < 4; ++s) acc = mfma(fuv[n][s], as_frag(bb[s]), acc);
          if (l15 < 4) {
            const v2u o = {cvtpk(acc[0], acc[1]), cvtpk(acc[2], acc[3])};
            *(LAS v2u*)(L + WL_CX + l15 * 272 + (16 * (2 * w + n) + 4 * g) * 2) = o;
          }
        }
      }
      team_bar(tb, 16, lane);
      {
        v4u bb[4];
#pragma unroll
        for (int ss = 0; ss < 4; ++ss)
          bb[ss] = *(LAS v4u*)(L + WL_CX + (l15 & 3) * 272 + (32 * ((ss + j) & 3) + 8 * g) * 2);
#pragma unroll
        for (int n = 0; n < 8; ++n) {
          f32x4 acc = {0, 0, 0, 0};
#pragma unroll
          for (int ss = 0; ss < 4; ++ss) acc = mfma(fo[ss][n], as_frag(bb[ss]), acc);
          if (l15 < 4) {
            const v4u o = {nosent(__float_as_uint(acc[0])), nosent(__float_as_uint(acc[1])), nosent(__float_as_uint(acc[2])), nosent(__float_as_uint(acc[3]))};
            ST128(o, WS_OUTP + (((l15 * 32 + 8 * w + n) * 128 + wi) * 16 + 4 * g) * 4);
          }
        }
      }
      STAMP(8);
    } else if (h < 28) {
      const int w4 = w - 4;
      LAS u32* fb_ = (LAS u32*)(L + WL_CNT + 4);
      {
        v4u t[8];
        const u8* base = (l15 < 8) ? p.wg : p.wu;
#pragma unroll
        for (int i = 0; i < 8; ++i)
          t[i] = ldnt(base + ((size_t)(128 * j + 32 * w4 + 4 * i + g) * 1792 + h * 64) * 2 + (l15 & 7) * 16);
        v4u d[4];
#pragma unroll
        for (int i = 0; i < 4; ++i)
          d[i] = ldnt(p.wd + ((size_t)(h * 64 + 16 * j + 4 * i + g) * 512 + 128 * w4) * 2 + l15 * 16);
#pragma unroll
        for (int i = 0; i < 8; ++i) img256_write(L + WL_IMG1, 32 * w4 + 4 * i, lane, t[i]);
#pragma unroll
        for (int i = 0; i < 4; ++i) img256_write(L + WL_SCRF + w4 * 4096, 4 * i, lane, d[i]);
      }
      team_bar(fb_, 4, lane);
      {
        f32x4 ag = {0, 0, 0, 0}, au = {0, 0, 0, 0};
#pragma unroll
        for (int s = 0; s < 4; ++s) {
          const v4u b = *(LAS v4u*)(L + WL_XN + (l15 & 3) * 1040 + (128 * j + 32 * s + 8 * g) * 2);
          ag = mfma(tr_frag256(L + WL_IMG1, 32 * s, 31, w4, lane), as_frag(b), ag);
          au = mfma(tr_frag256(L + WL_IMG1, 32 * s, 31, 4 + w4, lane), as_frag(b), au);
        }
        if (l15 < 4) {
          const u32 off = WS_FP + ((((h * 4 + j) * 2 + 0) * 4 + l15) * 64 + 16 * w4 + 4 * g) * 8;
          const v4u a0 = {__float_as_uint(ag[0]), tag, __float_as_uint(ag[1]), tag};
          const v4u a1 = {__float_as_uint(ag[2]), tag, __float_as_uint(ag[3]), tag};
          const v4u b0 = {__float_as_uint(au[0]), tag, __float_as_uint(au[1]), tag};
          const v4u b1 = {__float_as_uint(au[2]), tag, __float_as_uint(au[3]), tag};
          ST128(a0, off); ST128(a1, off + 16); ST128(b0, off + 2048); ST128(b1, off + 2048 + 16);
        }
      }
      bf16x8 fd[8];
#pragma unroll
      for (int n = 0; n < 8; ++n) fd[n] = tr_frag256(L + WL_SCRF + w4 * 4096, 0, 15, n, lane);
      if (w4 == 0) {
        const int b = g, f = l15;
        const u32 off = WS_FP + ((h * 8) * 4 + b) * 512 + (16 * j + f) * 8;
        float gs, us;
        for (u32 spin = 0;; ++spin) {
          v2u vg[4], vu[4];
#pragma unroll
          for (int s = 0; s < 4; ++s) { vg[s] = LD64(off + s * 4096); vu[s] = LD64(off + s * 4096 + 2048); }
          bool ok = true;
#pragma unroll
          for (int s = 0; s < 4; ++s) ok &= (vg[s].y == tag) & (vu[s].y == tag);
          gs = (__uint_as_float(vg[0].x) + __uint_as_float(vg[1].x)) + (__uint_as_float(vg[2].x) + __uint_as_float(vg[3].x));
          us = (__uint_as_float(vu[0].x) + __uint_as_float(vu[1].x)) + (__uint_as_float(vu[2].x) + __uint_as_float(vu[3].x));
          if (__all(ok) || spin > SPIN_CAP) break;
          __builtin_amdgcn_s_sleep(1);
        }
        const float hv = gs / (1.f + __expf(-gs)) * us;
        *(LAS u16*)(L + WL_HB + b * 80 + f * 2) = (u16)cvtpk(hv, 0.f);
        *(LAS u16*)(L + WL_HB + b * 80 + 32 + f * 2) = 0;
      }
      team_bar(fb_, 8, lane);
      f32x4 y[8];
      {
        const v4u bb = *(LAS v4u*)(L + WL_HB + (l15 & 3) * 80 + (8 * g) * 2);
#pragma unroll
        for (int n = 0; n < 8; ++n) { f32x4 z = {0, 0, 0, 0}; y[n] = mfma(fd[n], as_frag(bb), z); }
      }
      for (u32 spin = 0; __hip_atomic_load((LAS u32*)(L + WL_CNT + 8), __ATOMIC_RELAXED, __HIP_MEMORY_SCOPE_WORKGROUP) == 0 && spin < (SPIN_CAP << 6); ++spin) __builtin_amdgcn_s_sleep(1);
      asm volatile("" ::: "memory");
      if (l15 < 4) {
        const int src = h * 4 + j;
#pragma unroll
        for (int n = 0; n < 8; ++n) {
          const v4u o = {nosent(__float_as_uint(y[n][0])), nosent(__float_as_uint(y[n][1])), nosent(__float_as_uint(y[n][2])), nosent(__float_as_uint(y[n][3]))};
          ST128(o, WS_YP + (((l15 * 32 + 8 * w4 + n) * 112 + src) * 16 + 4 * g) * 4);
        }
      }
    }
  } else {
    const int a = xcd * 16 + (rr - 16), b = a >> 5, ch = a & 31;
    if (w < 4) {
#define CKWIN 10
      constexpr int WIN = CKWIN;
      const u8* src = p.ckv + ((size_t)(b * 4096 + ch * 128) * 512) * 2 + lane * 16;
      v4u r[WIN];
#pragma unroll
      for (int i = 0; i < WIN; ++i) r[i] = ldnt(src + (4 * i + w) * 1024);
      __builtin_amdgcn_sched_barrier(0);
#pragma unroll
      for (int i = 0; i < 32; ++i) {
        const v4u v = r[i % WIN];
        const u32 key = 4 * i + w;
        *(LAS v4u*)(L + AL_KV + key * 1024 + (((u32)(lane & 48) | ((u32)l15 ^ swz16(key & 15))) << 4)) = v;
        if (i + WIN < 32) r[i % WIN] = ldnt(src + (4 * (i + WIN) + w) * 1024);
        __builtin_amdgcn_sched_barrier(0);
      }
      STAMP(1);
    } else if (w == 4) {
      const v4u sv = {SENT, SENT, SENT, SENT};
#pragma unroll
      for (int i = 0; i < 8; ++i) ST128(sv, WS_OUTP + a * 8192 + i * 1024 + lane * 16);
#pragma unroll
      for (int i = 0; i < 7; ++i) ST128(sv, WS_YP + a * 7168 + i * 1024 + lane * 16);
      GET_TAG();
      for (u32 spin = 0;; ++spin) {
        const v2u f = LD64(WS_QF + (a & 15) * 512 + lane * 8);
        if (__all((f.x == tag) & (f.y == tag)) || spin > SPIN_CAP) break;
        __builtin_amdgcn_s_sleep(4);
      }
      __builtin_amdgcn_fence(__ATOMIC_ACQUIRE, "wavefront");
#pragma unroll
      for (int q = 0; q < 2; ++q) {
        v4u t[16];
#pragma unroll
        for (int i = 0; i < 16; ++i) t[i] = LD128(WS_QA + b * 32768 + (q * 16 + i) * 1024 + lane * 16);
#pragma unroll
        for (int i = 0; i < 16; ++i) *(LAS v4u*)(L + AL_Q + (q * 16 + i) * 1024 + lane * 16) = t[i];
      }
      drain_vm();
    }
    wg_barrier();
    STAMP(4);
    f32x4 s0 = {0, 0, 0, 0}, s1 = {0, 0, 0, 0};
    {
      const u32 key = 16 * w + l15;
      const u32 sw = swz16(l15);
#pragma unroll
      for (int s = 0; s < 16; ++s) {
        const u32 chn = 4 * s + g;
        const v4u av = *(LAS v4u*)(L + AL_KV + key * 1024 + (((chn & 48) | ((chn & 15) ^ sw)) << 4));
        const v4u b0 = *(LAS v4u*)(L + AL_Q + (s * 64 + lane) * 16);
        const v4u b1 = *(LAS v4u*)(L + AL_Q + ((16 + s) * 64 + lane) * 16);
        s0 = mfma(as_frag(av), as_frag(b0), s0);
        s1 = mfma(as_frag(av), as_frag(b1), s1);
      }
    }
    float m0 = xrow16_max(fmaxf(fmaxf(s0[0], s0[1]), fmaxf(s0[2], s0[3])));
    float m1 = xrow16_max(fmaxf(fmaxf(s1[0], s1[1]), fmaxf(s1[2], s1[3])));
    wg_barrier();
    if (g == 0) {
      *(LAS float*)(L + AL_MX + (l15 * 8 + w) * 4) = m0;
      *(LAS float*)(L + AL_MX + ((16 + l15) * 8 + w) * 4) = m1;
    }
    wg_barrier();
    STAMP(5);
    float M0, M1;
    {
      const f32x4 a0 = *(LAS f32x4*)(L + AL_MX + l15 * 32), a1 = *(LAS f32x4*)(L + AL_MX + l15 * 32 + 16);
      const f32x4 c0 = *(LAS f32x4*)(L + AL_MX + (16 + l15) * 32), c1 = *(LAS f32x4*)(L + AL_MX + (16 + l15) * 32 + 16);
      M0 = fmaxf(fmaxf(fmaxf(a0[0], a0[1]), fmaxf(a0[2], a0[3])), fmaxf(fmaxf(a1[0], a1[1]), fmaxf(a1[2], a1[3])));
      M1 = fmaxf(fmaxf(fmaxf(c0[0], c0[1]), fmaxf(c0[2], c0[3])), fmaxf(fmaxf(c1[0], c1[1]), fmaxf(c1[2], c1[3])));
    }
    {
      const u32 p00 = cvtpk(__builtin_amdgcn_exp2f(s0[0] - M0), __builtin_amdgcn_exp2f(s0[1] - M0));
      const u32 p01 = cvtpk(__builtin_amdgcn_exp2f(s0[2] - M0), __builtin_amdgcn_exp2f(s0[3] - M0));
      const u32 p10 = cvtpk(__builtin_amdgcn_exp2f(s1[0] - M1), __builtin_amdgcn_exp2f(s1[1] - M1));
      const u32 p11 = cvtpk(__builtin_amdgcn_exp2f(s1[2] - M1), __builtin_amdgcn_exp2f(s1[3] - M1));
      const float l0 = xrow16_sum((bf_lo(p00) + bf_hi(p00)) + (bf_lo(p01) + bf_hi(p01)));
      const float l1 = xrow16_sum((bf_lo(p10) + bf_hi(p10)) + (bf_lo(p11) + bf_hi(p11)));
      const int ks = w >> 1, gg = 2 * (w & 1) + (g >> 1);
      const v2u o0 = {p00, p01}, o1 = {p10, p11};
      *(LAS v2u*)(L + AL_P + ((0 * 4 + ks) * 64 + gg * 16 + l15) * 16 + 8 * (g & 1)) = o0;
      *(LAS v2u*)(L + AL_P + ((1 * 4 + ks) * 64 + gg * 16 + l15) * 16 + 8 * (g & 1)) = o1;
      if (g == 0) {
        *(LAS float*)(L + AL_LS + (l15 * 8 + w) * 4) = l0;
        *(LAS float*)(L + AL_LS + ((16 + l15) * 8 + w) * 4) = l1;
      }
    }
    wg_barrier();
    STAMP(6);
    if (w == 7) {
      const int hd = lane & 31;
      const f32x4 a0 = *(LAS f32x4*)(L + AL_MX + hd * 32), a1 = *(LAS f32x4*)(L + AL_MX + hd * 32 + 16);
      const f32x4 c0 = *(LAS f32x4*)(L + AL_LS + hd * 32), c1 = *(LAS f32x4*)(L + AL_LS + hd * 32 + 16);
      const float M = fmaxf(fmaxf(fmaxf(a0[0], a0[1]), fmaxf(a0[2], a0[3])), fmaxf(fmaxf(a1[0], a1[1]), fmaxf(a1[2], a1[3])));
      const float Ls = ((c0[0] + c0[1]) + (c0[2] + c0[3])) + ((c1[0] + c1[1]) + (c1[2] + c1[3]));
      const v2u o = {nosent(__float_as_uint(M)), nosent(__float_as_uint(Ls))};
      const int j0 = (lane >> 5) * 2;
      ST64(o, WS_ST + (hd * 4 + j0) * 1024 + a * 8);
      ST64(o, WS_ST + (hd * 4 + j0 + 1) * 1024 + a * 8);
    }
    {
      v4u pb[2][4];
#pragma unroll
      for (int ht = 0; ht < 2; ++ht)
#pragma unroll
        for (int ks = 0; ks < 4; ++ks) pb[ht][ks] = *(LAS v4u*)(L + AL_P + ((ht * 4 + ks) * 64 + lane) * 16);
      const int q = l15 >> 2, pp = l15 & 3;
      f32x4 acc[4][2];
#pragma unroll
      for (int ct = 0; ct < 4; ++ct) {
        acc[ct][0] = f32x4{0, 0, 0, 0}; acc[ct][1] = f32x4{0, 0, 0, 0};
        const u32 chn = (u32)(8 * w + 2 * ct + (pp >> 1));
#pragma unroll
        for (int ks = 0; ks < 4; ++ks) {
          const u32 k0 = 32 * ks + 8 * g + q, k1 = k0 + 4;
          const s16x4 x0 = __builtin_amdgcn_ds_read_tr16_b64_v4i16((LAS s16x4*)(L + AL_KV + k0 * 1024 + (((chn & 48) | ((chn & 15) ^ swz16(k0 & 15))) << 4) + 8 * (pp & 1)));
          const s16x4 x1 = __builtin_amdgcn_ds_read_tr16_b64_v4i16((LAS s16x4*)(L + AL_KV + k1 * 1024 + (((chn & 48) | ((chn & 15) ^ swz16(k1 & 15))) << 4) + 8 * (pp & 1)));
          const bf16x8 af = __builtin_shufflevector(x0, x1, 0, 1, 2, 3, 4, 5, 6, 7);
          acc[ct][0] = mfma(af, as_frag(pb[0][ks]), acc[ct][0]);
          acc[ct][1] = mfma(af, as_frag(pb[1][ks]), acc[ct][1]);
        }
      }
#pragma unroll
      for (int ht = 0; ht < 2; ++ht)
#pragma unroll
        for (int T = 0; T < 2; ++T) {
          v4u o;
          o.x = nosent(cvtpk(acc[2 * T][ht][0], acc[2 * T][ht][1])); o.y = nosent(cvtpk(acc[2 * T][ht][2], acc[2 * T][ht][3]));
          o.z = nosent(cvtpk(acc[2 * T + 1][ht][0], acc[2 * T + 1][ht][1])); o.w = nosent(cvtpk(acc[2 * T + 1][ht][2], acc[2 * T + 1][ht][3]));
          ST128(o, WS_PART + ((16 * ht + l15) * 4 + (w >> 1)) * 32768 + (a * 128 + 64 * (w & 1) + 32 * T + 8 * g) * 2);
        }
    }
    STAMP(7);
    {
      const int src = tid >> 2, qd = tid & 3;
      v4u vy = {0, 0, 0, 0}, vs;
      for (u32 spin = 0;; ++spin) {
        bool ok = true;
        if (src < 112) { vy = LD128(WS_YP + a * 7168 + tid * 16); ok &= (vy.x != SENT) & (vy.y != SENT) & (vy.z != SENT) & (vy.w != SENT); }
        vs = LD128(WS_OUTP + a * 8192 + tid * 16);
        ok &= (vs.x != SENT) & (vs.y != SENT) & (vs.z != SENT) & (vs.w != SENT);
        if (__all(ok) || spin > SPIN_CAP) break;
        __builtin_amdgcn_s_sleep(2);
      }
      STAMP(8);
      float r[8] = {__uint_as_float(vs.x), __uint_as_float(vs.y), __uint_as_float(vs.z), __uint_as_float(vs.w),
                    __uint_as_float(vy.x), __uint_as_float(vy.y), __uint_as_float(vy.z), __uint_as_float(vy.w)};
#pragma unroll
      for (int i = 0; i < 8; ++i) { float v = r[i]; v += dpp<0x124>(v); v += dpp<0x128>(v); r[i] = xrow16_sum(v); }
      if (lane < 4) {
        *(LAS f32x4*)(L + AL_RED + (w * 2 + 0) * 64 + qd * 16) = f32x4{r[0], r[1], r[2], r[3]};
        *(LAS f32x4*)(L + AL_RED + (w * 2 + 1) * 64 + qd * 16) = f32x4{r[4], r[5], r[6], r[7]};
      }
      wg_barrier();
      if (tid < 16) {
        float s = 0.f, y = 0.f;
#pragma unroll
        for (int ww = 0; ww < 8; ++ww) {
          s += *(LAS float*)(L + AL_RED + (ww * 2 + 0) * 64 + tid * 4);
          y += *(LAS float*)(L + AL_RED + (ww * 2 + 1) * 64 + tid * 4);
        }
        const int o = b * 512 + ch * 16 + tid;
        const float xv = bf_lo((u32) * (const GAS u16*)(p.x + o * 2));
        const float gt = y / (1.f + __expf(-y));
        *(GAS u16*)(p.out + o * 2) = (u16)cvtpk(xv + s * gt, 0.f);
      }
      if (a == 0 && tid == 0) { GET_TAG(); ST32(tag, WS_EPOCH); }
      STAMP(9);
    }
  }
}

extern "C" void kernel_launch(void* const* d_in, const int* in_sizes, int n_in, void* d_out, int out_size, void* d_ws, size_t ws_size,
                              hipStream_t stream) {
  Params p;
  p.x = (const u8*)d_in[0]; p.ckv = (const u8*)d_in[1]; p.wq = (const u8*)d_in[2]; p.wuk = (const u8*)d_in[3];
  p.wuv = (const u8*)d_in[4]; p.wo = (const u8*)d_in[5]; p.wg = (const u8*)d_in[7]; p.wu = (const u8*)d_in[8]; p.wd = (const u8*)d_in[9];
  p.out = (u8*)d_out; p.ws = (u8*)d_ws;
  static bool attr = false;
  if (!attr) { (void)hipFuncSetAttribute((const void*)mla_ffn_kernel, hipFuncAttributeMaxDynamicSharedMemorySize, LDS_BYTES); attr = true; }
  hipLaunchKernelGGL(mla_ffn_kernel, dim3(NWG), dim3(NTHR), LDS_BYTES, stream, p);
}
```

```cpp
#include <hip/hip_runtime.h>
#include <stdint.h>

typedef unsigned char u8;
typedef unsigned short u16;
typedef unsigned int u32;
typedef unsigned long long u64;
typedef u32 v4u __attribute__((ext_vector_type(4)));
typedef u32 v2u __attribute__((ext_vector_type(2)));
typedef float f32x4 __attribute__((ext_vector_type(4)));
typedef short bf16x8 __attribute__((ext_vector_type(8)));
typedef short s16x4 __attribute__((ext_vector_type(4)));
#define GAS __attribute__((address_space(1)))
#define LAS __attribute__((address_space(3)))
#define DI __device__ __forceinline__

constexpr int NWG = 256, NTHR = 512;
constexpr u32 SENT = 0xFFFFFFFFu;
constexpr int LDS_BYTES = 163840;

constexpr u32 WS_EPOCH = 0;
constexpr u32 WS_QF = 1024;
constexpr u32 WS_QA = 16384;
constexpr u32 WS_QP = WS_QA + 4 * 32768;
constexpr u32 WS_FP = WS_QP + 128 * 2048;
constexpr u32 WS_ST = WS_FP + 112 * 2048;
constexpr u32 WS_OUTP = WS_ST + 128 * 2048;
constexpr u32 WS_YP = WS_OUTP + 64 * 16384;
constexpr u32 WS_PART = WS_YP + 64 * 14336;
constexpr u32 WS_END = WS_PART + 128 * 32768;
constexpr u32 WS_STAMP = 200u << 20;

constexpr int AUX_SC1 = 16;
constexpr u32 SPIN_CAP = 1u << 17;
#define POLLSLEEP 8
#define CLRSLEEP 100
#define CKWIN 6

#define STAMPW(k, wv) do { } while (0)
#define STAMP_FLUSH() do { } while (0)
#define STAMP(k) STAMPW(k, 0)

struct Params {
  const u8* x; const u8* ckv; const u8* wq; const u8* wuk; const u8* wuv; const u8* wo;
  const u8* wg; const u8* wu; const u8* wd; u8* out; u8* ws;
};

DI float bf_lo(u32 v) { return __uint_as_float(v << 16); }
DI float bf_hi(u32 v) { return __uint_as_float(v & 0xFFFF0000u); }
DI u32 cvtpk(float lo, float hi) {
  typedef __bf16 bf2 __attribute__((ext_vector_type(2)));
  typedef float f2 __attribute__((ext_vector_type(2)));
  f2 f = {lo, hi};
  bf2 r = __builtin_convertvector(f, bf2);
  return __builtin_bit_cast(u32, r);
}
DI u32 nosent(u32 v) { return v < 0xFFFFFFFEu ? v : 0xFFFFFFFEu; }
DI v4u ldnt(const u8* p) { return __builtin_nontemporal_load((const GAS v4u*)p); }
DI f32x4 mfma(v4u a, v4u b, f32x4 c) {
  return __builtin_amdgcn_mfma_f32_16x16x32_bf16(__builtin_bit_cast(bf16x8, a), __builtin_bit_cast(bf16x8, b), c, 0, 0, 0);
}
DI u32 swz16(u32 k) { const u32 k2 = (k >> 2) & 1u, k3 = (k >> 3) & 1u; return k2 | ((k & 3u) << 1) | ((k2 ^ k3) << 3); }
DI void wg_barrier() { asm volatile("s_waitcnt lgkmcnt(0)\n\ts_barrier" ::: "memory"); }
DI void drain_vm() { asm volatile("s_waitcnt vmcnt(0)" ::: "memory"); }
DI float xrow16_max(float x) {
  auto s = __builtin_amdgcn_permlane16_swap(__float_as_uint(x), __float_as_uint(x), false, false);
  x = fmaxf(__uint_as_float(s[0]), __uint_as_float(s[1]));
  auto t = __builtin_amdgcn_permlane32_swap(__float_as_uint(x), __float_as_uint(x), false, false);
  return fmaxf(__uint_as_float(t[0]), __uint_as_float(t[1]));
}
DI float xrow16_sum(float x) {
  auto s = __builtin_amdgcn_permlane16_swap(__float_as_uint(x), __float_as_uint(x), false, false);
  x = __uint_as_float(s[0]) + __uint_as_float(s[1]);
  auto t = __builtin_amdgcn_permlane32_swap(__float_as_uint(x), __float_as_uint(x), false, false);
  return __uint_as_float(t[0]) + __uint_as_float(t[1]);
}
template <int CTRL> DI float dpp(float x) {
  return __builtin_bit_cast(float, __builtin_amdgcn_mov_dpp(__builtin_bit_cast(int, x), CTRL, 0xf, 0xf, true));
}
DI float row16_sum(float v) {
  v += dpp<0xB1>(v); v += dpp<0x4E>(v); v += dpp<0x124>(v); v += dpp<0x128>(v);
  return v;
}
DI float wave_sum(float v) { return xrow16_sum(row16_sum(v)); }
DI void team_bar(LAS u32* cnt, u32 target, int lane) {
  asm volatile("s_waitcnt lgkmcnt(0)" ::: "memory");
  if (lane == 0) __hip_atomic_fetch_add(cnt, 1u, __ATOMIC_RELAXED, __HIP_MEMORY_SCOPE_WORKGROUP);
  while (__hip_atomic_load(cnt, __ATOMIC_RELAXED, __HIP_MEMORY_SCOPE_WORKGROUP) < target) { }
  asm volatile("" ::: "memory");
}
struct Sel { u32 s[2][4]; };
DI Sel make_sel(int l15) {
  Sel r;
  const int jj = l15 & 7;
#pragma unroll
  for (int par = 0; par < 2; ++par) {
    const u32 hb = par ? 0x0302u : 0x0100u;
    const u32 v = (jj & 1) ? ((hb << 16) | 0x0C0Cu) : (0x0C0C0000u | hb);
#pragma unroll
    for (int k = 0; k < 4; ++k) r.s[par][k] = ((jj >> 1) == k) ? v : 0x0C0C0C0Cu;
  }
  return r;
}
DI v4u diagA(const Sel& sel, u32 src, int par) {
  v4u a;
  a.x = __builtin_amdgcn_perm(0u, src, sel.s[par][0]); a.y = __builtin_amdgcn_perm(0u, src, sel.s[par][1]);
  a.z = __builtin_amdgcn_perm(0u, src, sel.s[par][2]); a.w = __builtin_amdgcn_perm(0u, src, sel.s[par][3]);
  return a;
}

#define LD128(off) __builtin_amdgcn_raw_buffer_load_b128(rs, (off), 0, AUX_SC1)
#define LD64(off) __builtin_amdgcn_raw_buffer_load_b64(rs, (off), 0, AUX_SC1)
#define ST128(v, off) __builtin_amdgcn_raw_buffer_store_b128((v), rs, (off), 0, AUX_SC1)
#define ST64(v, off) __builtin_amdgcn_raw_buffer_store_b64((v), rs, (off), 0, AUX_SC1)
#define ST32(v, off) __builtin_amdgcn_raw_buffer_store_b32((v), rs, (off), 0, AUX_SC1)

DI bf16x8 tr_frag256(LAS u8* img, int row_base  , int rowmask, int nt, int lane) {
  const int g = lane >> 4, i = lane & 15, q = i >> 2, pp = i & 3;
  const u32 r0 = (u32)(row_base + ((8 * g + q) & rowmask)), r1 = (u32)(row_base + ((8 * g + 4 + q) & rowmask));
  const u32 ch = 2 * nt + (pp >> 1), o8 = 8 * (pp & 1);
  const s16x4 a = __builtin_amdgcn_ds_read_tr16_b64_v4i16((LAS s16x4*)(img + r0 * 256 + ((ch ^ swz16(r0 & 15)) << 4) + o8));
  const s16x4 b = __builtin_amdgcn_ds_read_tr16_b64_v4i16((LAS s16x4*)(img + r1 * 256 + ((ch ^ swz16(r1 & 15)) << 4) + o8));
  return __builtin_shufflevector(a, b, 0, 1, 2, 3, 4, 5, 6, 7);
}
DI void img256_write(LAS u8* img, int r0, int lane, v4u v) {
  const u32 row = (u32)(r0 + (lane >> 4));
  *(LAS v4u*)(img + row * 256 + (((u32)(lane & 15) ^ swz16(row & 15)) << 4)) = v;
}
DI v4u fr(bf16x8 x) { return __builtin_bit_cast(v4u, x); }
constexpr int WL_XN = 0;
constexpr int WL_Q = 4224;
constexpr int WL_CC = 5376;
constexpr int WL_CX = 6528;
constexpr int WL_IMG = 8192;
static_assert(WL_IMG + 131072 <= LDS_BYTES, "lds");
constexpr int AL_KV = 0;
constexpr int AL_Q = 131072;
constexpr int AL_MX = AL_Q;
constexpr int AL_LS = AL_Q + 1024;
constexpr int AL_P = AL_Q + 2048;
constexpr int AL_XN = 0;
constexpr int AL_RED = 8192;
constexpr int AL_FV = 24576;
constexpr int AL_FH = 26624;
constexpr int AL_FR = 28672;

__global__ __launch_bounds__(NTHR, 2) void mla_ffn_kernel(Params p) {
  extern __shared__ __attribute__((aligned(16))) u8 smem[];
  LAS u8* L = (LAS u8*)smem;
  const int tid = threadIdx.x, lane = tid & 63;
  const int w = __builtin_amdgcn_readfirstlane(tid >> 6);
  const int bid = blockIdx.x, xcd = bid & 7, rr = bid >> 3;
  const __amdgpu_buffer_rsrc_t rs = __builtin_amdgcn_make_buffer_rsrc((void*)p.ws, 0, (int)WS_END, 0x00020000);
  const int g = lane >> 4, l15 = lane & 15;
  STAMP(0);
  u32 ep_raw = __builtin_amdgcn_raw_buffer_load_b32(rs, WS_EPOCH, 0, 0);
#define GET_TAG() u32 tag; { u32 e_ = ep_raw; asm volatile("" : "+v"(e_)); tag = e_ + 1u; }

#define WARM_LD(off) __builtin_amdgcn_raw_buffer_load_b32(rs, (off), 0, 0)
#define WARM_USE(x) asm volatile("" :: "v"(x))
  u32 wm0 = 0, wm1 = 0, wm2 = 0;
  if (rr < 16) {
    const int h = xcd * 4 + (rr >> 2), j = rr & 3, wi = h * 4 + j;
    v4u xv = {0, 0, 0, 0};
    if (w < 4) xv = *(const GAS v4u*)(p.x + w * 1024 + lane * 16);
    if (w == 4) wm0 = WARM_LD(WS_QP + (wi & ~3) * 2048 + lane * 128);
    if (w == 5) wm0 = WARM_LD(lane < 32 ? WS_QA + ((lane >> 3) * 32 + h) * 1024 + (lane & 7) * 128 : WS_QF + ((lane - 32) + 32 * (wi & 1)) * 128);
    v4u wqv[4];
#pragma unroll
    for (int i = 0; i < 4; ++i) wqv[i] = ldnt(p.wq + ((size_t)(128 * j + 16 * w + 4 * i + g) * 4096 + h * 128) * 2 + l15 * 16);
    __builtin_amdgcn_sched_barrier(0);
    v4u wuk[4];
    if (w < 4) {
      float v[8] = {bf_lo(xv.x), bf_hi(xv.x), bf_lo(xv.y), bf_hi(xv.y), bf_lo(xv.z), bf_hi(xv.z), bf_lo(xv.w), bf_hi(xv.w)};
      float s = 0.f;
#pragma unroll
      for (int i = 0; i < 8; ++i) s += v[i];
      const float mean = wave_sum(s) * (1.f / 512.f);
      float q2 = 0.f;
#pragma unroll
      for (int i = 0; i < 8; ++i) { v[i] -= mean; q2 += v[i] * v[i]; }
      const float rstd = __builtin_amdgcn_rsqf(wave_sum(q2) * (1.f / 512.f) + 1e-5f);
      v4u o;
      o.x = cvtpk(v[0] * rstd, v[1] * rstd); o.y = cvtpk(v[2] * rstd, v[3] * rstd);
      o.z = cvtpk(v[4] * rstd, v[5] * rstd); o.w = cvtpk(v[6] * rstd, v[7] * rstd);
      *(LAS v4u*)(L + WL_XN + w * 1040 + lane * 16) = o;
    }
#pragma unroll
    for (int i = 0; i < 4; ++i)
      wuk[i] = ldnt(p.wuk + ((size_t)(128 * j + 16 * w + 4 * i + g) * 4096 + h * 128) * 2 + l15 * 16);
    __builtin_amdgcn_sched_barrier(0);
#pragma unroll
    for (int i = 0; i < 4; ++i) img256_write(L + WL_IMG, 16 * w + 4 * i, lane, wqv[i]);
    wg_barrier();
    STAMP(1);
    GET_TAG();
    {
      f32x4 acc = {0, 0, 0, 0};
#pragma unroll
      for (int s = 0; s < 4; ++s) {
        const bf16x8 a = tr_frag256(L + WL_IMG, 32 * s, 31, w, lane);
        const v4u b = *(LAS v4u*)(L + WL_XN + (l15 & 3) * 1040 + (128 * j + 32 * s + 8 * g) * 2);
        acc = mfma(fr(a), b, acc);
      }
      if (l15 < 4) {
        const v2u o = {cvtpk(acc[0], acc[1]), cvtpk(acc[2], acc[3])};
        *(LAS v2u*)(L + WL_CC + l15 * 272 + (16 * w + 4 * g) * 2) = o;
      }
      wg_barrier();
      if (w < 2) {
        const u32 rel = (2 * w + (lane >> 5)) * 512 + (lane & 31) * 16;
        {
          const v2u o = *(LAS v2u*)(L + WL_CC + (2 * w + (lane >> 5)) * 272 + (lane & 31) * 8);
          const v4u o4 = {o.x, tag, o.y, tag};
          ST128(o4, WS_QP + wi * 2048 + rel);
        }
        float t[4];
        for (u32 spin = 0;; ++spin) {
          v4u v[4];
#pragma unroll
          for (int q = 0; q < 4; ++q) v[q] = LD128(WS_QP + (h * 4 + q) * 2048 + rel);
          bool ok = true;
          t[0] = t[1] = t[2] = t[3] = 0.f;
#pragma unroll
          for (int q = 0; q < 4; ++q) {
            ok &= (v[q].y == tag) & (v[q].w == tag);
            t[0] += bf_lo(v[q].x); t[1] += bf_hi(v[q].x); t[2] += bf_lo(v[q].z); t[3] += bf_hi(v[q].z);
          }
          if (__all(ok) || spin > SPIN_CAP) break;
          __builtin_amdgcn_s_sleep(1);
        }
        const v2u o2 = {cvtpk(t[0], t[1]), cvtpk(t[2], t[3])};
        *(LAS v2u*)(L + WL_Q + (2 * w + (lane >> 5)) * 272 + (lane & 31) * 8) = o2;
      }
    }
    v4u ukf[4];
    {
      LAS u8* im = L + WL_IMG + 32768 + w * 4096;
#pragma unroll
      for (int i = 0; i < 4; ++i) img256_write(im, 4 * i, lane, wuk[i]);
#pragma unroll
      for (int s = 0; s < 4; ++s) ukf[s] = *(LAS v4u*)(im + l15 * 256 + (((u32)(4 * s + g) ^ swz16(l15)) << 4));
    }
    WARM_USE(wm0);
    wg_barrier();
    STAMP(2);
    {
      f32x4 acc = {0, 0, 0, 0};
#pragma unroll
      for (int s = 0; s < 4; ++s) acc = mfma(ukf[s], *(LAS v4u*)(L + WL_Q + (l15 & 3) * 272 + (32 * s + 8 * g) * 2), acc);
      constexpr float SC = 0.08838834764831845f * 1.4426950408889634f;
      if (l15 < 4) {
        const v2u o = {cvtpk(acc[0] * SC, acc[1] * SC), cvtpk(acc[2] * SC, acc[3] * SC)};
        *(LAS v2u*)(L + WL_CX + l15 * 272 + (16 * w + 4 * g) * 2) = o;
      }
    }
    wg_barrier();
    if (w == 0) {
      const v4u o = *(LAS v4u*)(L + WL_CX + g * 272 + l15 * 16);
      ST128(o, WS_QA + (g * 32 + h) * 1024 + j * 256 + l15 * 16);
      drain_vm();
      if (lane < 16) ST32(tag, WS_QF + lane * 512 + wi * 4);
    }
    STAMP(3);
    if (w == 6) { wm1 = WARM_LD(lane < 16 ? WS_ST + wi * 2048 + lane * 128 : WS_FP + (wi < 112 ? wi : 0) * 2048 + (lane & 15) * 128); }
    if (w == 7) { wm1 = WARM_LD(WS_OUTP + wi * 8192 + lane * 128); wm2 = WARM_LD(WS_YP + wi * 7168 + (lane < 56 ? lane : 0) * 128); }
    bf16x8 fuv[4], fo[4][4];
    {
      v4u t[4];
#pragma unroll
      for (int i = 0; i < 4; ++i)
        t[i] = ldnt(p.wuv + ((size_t)(128 * j + 16 * w + 4 * i + g) * 4096 + h * 128) * 2 + l15 * 16);
      v4u u[4][4];
#define WO_ISSUE(ss) _Pragma("unroll") for (int i = 0; i < 4; ++i) u[ss][i] = ldnt(p.wo + ((size_t)(h * 128 + 32 * (((ss) + j) & 3) + 4 * w + i) * 512) * 2 + lane * 16); __builtin_amdgcn_sched_barrier(0);
      WO_ISSUE(0)
#pragma unroll
      for (int i = 0; i < 4; ++i) img256_write(L + WL_IMG, 16 * w + 4 * i, lane, t[i]);
      wg_barrier();
#pragma unroll
      for (int s = 0; s < 4; ++s) fuv[s] = tr_frag256(L + WL_IMG, 32 * s, 31, w, lane);
#pragma unroll
      for (int ss = 0; ss < 4; ++ss) {
        if (ss == 0) { WO_ISSUE(1) }
        if (ss == 1) { WO_ISSUE(2) }
        if (ss == 2) { WO_ISSUE(3) }
        LAS u8* img = L + WL_IMG + ((ss + 1) & 3) * 32768;
        if (ss == 3) wg_barrier();
#pragma unroll
        for (int i = 0; i < 4; ++i) {
          const u32 row = 4 * w + i;
          *(LAS v4u*)(img + g * 8192 + row * 256 + (((u32)l15 ^ swz16(row & 15)) << 4)) = u[ss][i];
        }
        wg_barrier();
#pragma unroll
        for (int n = 0; n < 4; ++n) fo[ss][n] = tr_frag256(img + (w >> 1) * 8192, 0, 31, 4 * (w & 1) + n, lane);
      }
    }
    WARM_USE(wm1); WARM_USE(wm2);
    STAMP(4);
    if (w < 4) {
      const int b = w;
      u32 asc[8];
      float invL;
      {
        float m[8], lsum[8];
        for (u32 spin = 0;; ++spin) {
          bool ok = true;
#pragma unroll
          for (int s = 0; s < 8; ++s) {
            const v4u v = LD128(WS_ST + wi * 2048 + (b * 32 + 4 * s + g) * 16);
            ok &= (v.y == tag) & (v.w == tag);
            m[s] = __uint_as_float(v.x); lsum[s] = __uint_as_float(v.z);
          }
          if (__all(ok) || spin > SPIN_CAP) break;
          __builtin_amdgcn_s_sleep(4);
        }
        float M = m[0];
#pragma unroll
        for (int s = 1; s < 8; ++s) M = fmaxf(M, m[s]);
        M = xrow16_max(M);
        float Ls = 0.f;
#pragma unroll
        for (int s = 0; s < 8; ++s) {
          const u32 e = cvtpk(__builtin_amdgcn_exp2f(m[s] - M), 0.f) & 0xFFFFu;
          asc[s] = e;
          Ls += bf_lo(e) * lsum[s];
        }
        Ls = xrow16_sum(Ls);
        invL = __builtin_amdgcn_rcpf(Ls);
      }
      __builtin_amdgcn_fence(__ATOMIC_ACQUIRE, "wavefront");
      STAMP(5);
      v4u v[8];
      for (u32 spin = 0;; ++spin) {
        bool ok = true;
#pragma unroll
        for (int s = 0; s < 8; ++s) {
          v[s] = LD128(WS_PART + wi * 32768 + ((b * 32 + 4 * s + g) * 128) * 2 + l15 * 16);
          ok &= (v[s].x != SENT) & (v[s].y != SENT) & (v[s].z != SENT) & (v[s].w != SENT);
        }
        if (__all(ok) || spin > SPIN_CAP) break;
        __builtin_amdgcn_s_sleep(1);
      }
      STAMP(6);
      f32x4 acc = {0, 0, 0, 0};
#pragma unroll
      for (int s = 0; s < 8; ++s) {
        v4u a = diagA(make_sel(l15), asc[s], 0);
        if (l15 >= 8) a = v4u{0, 0, 0, 0};
        acc = mfma(a, v[s], acc);
      }
      if (g < 2) {
        const v2u o = {cvtpk(acc[0] * invL, acc[1] * invL), cvtpk(acc[2] * invL, acc[3] * invL)};
        *(LAS v2u*)(L + WL_CC + b * 272 + (32 * (l15 >> 2) + 16 * g + 4 * (l15 & 3)) * 2) = o;
      }
    }
    wg_barrier();
    {
      f32x4 acc = {0, 0, 0, 0};
#pragma unroll
      for (int s = 0; s < 4; ++s) acc = mfma(fr(fuv[s]), *(LAS v4u*)(L + WL_CC + (l15 & 3) * 272 + (32 * s + 8 * g) * 2), acc);
      if (l15 < 4) {
        const v2u o = {cvtpk(acc[0], acc[1]), cvtpk(acc[2], acc[3])};
        *(LAS v2u*)(L + WL_CX + l15 * 272 + (16 * w + 4 * g) * 2) = o;
      }
    }
    wg_barrier();
    {
      v4u bb[4];
#pragma unroll
      for (int ss = 0; ss < 4; ++ss) bb[ss] = *(LAS v4u*)(L + WL_CX + (l15 & 3) * 272 + (32 * ((ss + j) & 3) + 8 * g) * 2);
      f32x4 acc[4];
#pragma unroll
      for (int n = 0; n < 4; ++n) {
        acc[n] = f32x4{0, 0, 0, 0};
#pragma unroll
        for (int ss = 0; ss < 4; ++ss) acc[n] = mfma(fr(fo[ss][n]), bb[ss], acc[n]);
      }
      const int u = l15 >> 2;
      const f32x4 a = u == 0 ? acc[0] : (u == 1 ? acc[1] : (u == 2 ? acc[2] : acc[3]));
      const v4u o = {cvtpk(a[0], a[1]), tag, cvtpk(a[2], a[3]), tag};
      ST128(o, WS_OUTP + (((l15 & 3) * 16 + 2 * w + (u >> 1)) * 128 + wi) * 128 + ((u & 1) * 4 + g) * 16);
    }
    STAMP(7);
    STAMP_FLUSH();
  } else {
    const int a = xcd * 16 + (rr - 16), b = a >> 5, ch = a & 31;
    v4u xv = {0, 0, 0, 0};
    if (w < 4) {
      constexpr int WIN = CKWIN;
      const u8* src = p.ckv + ((size_t)(b * 4096 + ch * 128) * 512) * 2 + lane * 16;
      v4u r[WIN];
#pragma unroll
      for (int i = 0; i < WIN; ++i) r[i] = ldnt(src + (4 * i + w) * 1024);
      __builtin_amdgcn_sched_barrier(0);
#pragma unroll
      for (int i = 0; i < 32; ++i) {
        const v4u v = r[i % WIN];
        const u32 key = 4 * i + w;
        *(LAS v4u*)(L + AL_KV + key * 1024 + (((u32)(lane & 48) | ((u32)l15 ^ swz16(key & 15))) << 4)) = v;
        if (i + WIN < 32) r[i % WIN] = ldnt(src + (4 * (i + WIN) + w) * 1024);
        __builtin_amdgcn_sched_barrier(0);
      }
      STAMP(1);
      xv = *(const GAS v4u*)(p.x + w * 1024 + lane * 16);
    } else if (w == 4) {
      GET_TAG();
      for (u32 spin = 0;; ++spin) {
        const v2u f = LD64(WS_QF + (a & 15) * 512 + lane * 8);
        if (__all((f.x == tag) & (f.y == tag)) || spin > SPIN_CAP) break;
        __builtin_amdgcn_s_sleep(POLLSLEEP);
      }
      __builtin_amdgcn_fence(__ATOMIC_ACQUIRE, "wavefront");
      STAMPW(2, 4);
#pragma unroll
      for (int q = 0; q < 2; ++q) {
        v4u t[16];
#pragma unroll
        for (int i = 0; i < 16; ++i) t[i] = LD128(WS_QA + b * 32768 + (q * 16 + i) * 1024 + lane * 16);
#pragma unroll
        for (int i = 0; i < 16; ++i) *(LAS v4u*)(L + AL_Q + (q * 16 + i) * 1024 + (((u32)(lane & 48) | ((u32)l15 ^ swz16(i))) << 4)) = t[i];
      }
      STAMPW(3, 4);
    } else if (w == 5) {
      const v4u sv = {SENT, SENT, SENT, SENT};
      __builtin_amdgcn_s_sleep(CLRSLEEP);
#pragma unroll
      for (int i = 0; i < 32; ++i) ST128(sv, WS_PART + (4 * i + g) * 32768 + a * 256 + l15 * 16);
      drain_vm();
      STAMPW(10, 5);
    }
    wg_barrier();
    STAMP(4);
    const int fb = a >> 2, ks4 = a & 3;
    const bool ffn = fb < 28;
    v4u f1[4], f2[2];
    f32x4 s0 = {0, 0, 0, 0}, s1 = {0, 0, 0, 0};
    {
      const u32 key = 16 * w + l15;
      const u32 sw = swz16(l15);
#pragma unroll
      for (int s = 0; s < 16; ++s) {
        const u32 chn = 4 * s + g;
        const v4u av = *(LAS v4u*)(L + AL_KV + key * 1024 + (((chn & 48) | ((chn & 15) ^ sw)) << 4));
        const v4u b0 = *(LAS v4u*)(L + AL_Q + l15 * 1024 + (((chn & 48) | ((chn & 15) ^ sw)) << 4));
        const v4u b1 = *(LAS v4u*)(L + AL_Q + (16 + l15) * 1024 + (((chn & 48) | ((chn & 15) ^ sw)) << 4));
        s0 = mfma(av, b0, s0);
        s1 = mfma(av, b1, s1);
      }
    }
    float m0 = xrow16_max(fmaxf(fmaxf(s0[0], s0[1]), fmaxf(s0[2], s0[3])));
    float m1 = xrow16_max(fmaxf(fmaxf(s1[0], s1[1]), fmaxf(s1[2], s1[3])));
    if (ffn) {
      const u8* b1 = (l15 < 8 ? p.wg : p.wu) + ((size_t)(128 * ks4 + 16 * w + 4 * g) * 1792 + fb * 64 + (l15 & 7) * 8) * 2;
#pragma unroll
      for (int s = 0; s < 4; ++s) f1[s] = ldnt(b1 + s * 3584);
      __builtin_amdgcn_sched_barrier(0);
    }
    wg_barrier();
    if (g == 0) {
      *(LAS float*)(L + AL_MX + (l15 * 8 + w) * 4) = m0;
      *(LAS float*)(L + AL_MX + ((16 + l15) * 8 + w) * 4) = m1;
    }
    wg_barrier();
    STAMP(5);
    float M0, M1;
    {
      const f32x4 a0 = *(LAS f32x4*)(L + AL_MX + l15 * 32), a1 = *(LAS f32x4*)(L + AL_MX + l15 * 32 + 16);
      const f32x4 c0 = *(LAS f32x4*)(L + AL_MX + (16 + l15) * 32), c1 = *(LAS f32x4*)(L + AL_MX + (16 + l15) * 32 + 16);
      M0 = fmaxf(fmaxf(fmaxf(a0[0], a0[1]), fmaxf(a0[2], a0[3])), fmaxf(fmaxf(a1[0], a1[1]), fmaxf(a1[2], a1[3])));
      M1 = fmaxf(fmaxf(fmaxf(c0[0], c0[1]), fmaxf(c0[2], c0[3])), fmaxf(fmaxf(c1[0], c1[1]), fmaxf(c1[2], c1[3])));
    }
    {
      const u32 p00 = cvtpk(__builtin_amdgcn_exp2f(s0[0] - M0), __builtin_amdgcn_exp2f(s0[1] - M0));
      const u32 p01 = cvtpk(__builtin_amdgcn_exp2f(s0[2] - M0), __builtin_amdgcn_exp2f(s0[3] - M0));
      const u32 p10 = cvtpk(__builtin_amdgcn_exp2f(s1[0] - M1), __builtin_amdgcn_exp2f(s1[1] - M1));
      const u32 p11 = cvtpk(__builtin_amdgcn_exp2f(s1[2] - M1), __builtin_amdgcn_exp2f(s1[3] - M1));
      const float l0 = xrow16_sum((bf_lo(p00) + bf_hi(p00)) + (bf_lo(p01) + bf_hi(p01)));
      const float l1 = xrow16_sum((bf_lo(p10) + bf_hi(p10)) + (bf_lo(p11) + bf_hi(p11)));
      const int ks = w >> 1, gg = 2 * (w & 1) + (g >> 1);
      const v2u o0 = {p00, p01}, o1 = {p10, p11};
      *(LAS v2u*)(L + AL_P + ((0 * 4 + ks) * 64 + gg * 16 + l15) * 16 + 8 * (g & 1)) = o0;
      *(LAS v2u*)(L + AL_P + ((1 * 4 + ks) * 64 + gg * 16 + l15) * 16 + 8 * (g & 1)) = o1;
      if (g == 0) {
        *(LAS float*)(L + AL_LS + (l15 * 8 + w) * 4) = l0;
        *(LAS float*)(L + AL_LS + ((16 + l15) * 8 + w) * 4) = l1;
      }
    }
    wg_barrier();
    STAMP(6);
    GET_TAG();
    if (w == 7) {
      const int hd = lane & 31;
      const f32x4 a0 = *(LAS f32x4*)(L + AL_MX + hd * 32), a1 = *(LAS f32x4*)(L + AL_MX + hd * 32 + 16);
      const f32x4 c0 = *(LAS f32x4*)(L + AL_LS + hd * 32), c1 = *(LAS f32x4*)(L + AL_LS + hd * 32 + 16);
      const float M = fmaxf(fmaxf(fmaxf(a0[0], a0[1]), fmaxf(a0[2], a0[3])), fmaxf(fmaxf(a1[0], a1[1]), fmaxf(a1[2], a1[3])));
      const float Ls = ((c0[0] + c0[1]) + (c0[2] + c0[3])) + ((c1[0] + c1[1]) + (c1[2] + c1[3]));
      const v4u o = {__float_as_uint(M), tag, __float_as_uint(Ls), tag};
      const int j0 = (lane >> 5) * 2;
      ST128(o, WS_ST + (hd * 4 + j0) * 2048 + a * 16);
      ST128(o, WS_ST + (hd * 4 + j0 + 1) * 2048 + a * 16);
    }
    {
      v4u pb[2][4];
#pragma unroll
      for (int ht = 0; ht < 2; ++ht)
#pragma unroll
        for (int ks = 0; ks < 4; ++ks) pb[ht][ks] = *(LAS v4u*)(L + AL_P + ((ht * 4 + ks) * 64 + lane) * 16);
      const int q = l15 >> 2, pp = l15 & 3;
      f32x4 acc[4][2];
#pragma unroll
      for (int ct = 0; ct < 4; ++ct) {
        acc[ct][0] = f32x4{0, 0, 0, 0}; acc[ct][1] = f32x4{0, 0, 0, 0};
        const u32 chn = (u32)(8 * w + 2 * ct + (pp >> 1));
#pragma unroll
        for (int ks = 0; ks < 4; ++ks) {
          const u32 k0 = 32 * ks + 8 * g + q, k1 = k0 + 4;
          const s16x4 x0 = __builtin_amdgcn_ds_read_tr16_b64_v4i16((LAS s16x4*)(L + AL_KV + k0 * 1024 + (((chn & 48) | ((chn & 15) ^ swz16(k0 & 15))) << 4) + 8 * (pp & 1)));
          const s16x4 x1 = __builtin_amdgcn_ds_read_tr16_b64_v4i16((LAS s16x4*)(L + AL_KV + k1 * 1024 + (((chn & 48) | ((chn & 15) ^ swz16(k1 & 15))) << 4) + 8 * (pp & 1)));
          const bf16x8 af = __builtin_shufflevector(x0, x1, 0, 1, 2, 3, 4, 5, 6, 7);
          acc[ct][0] = mfma(fr(af), pb[0][ks], acc[ct][0]);
          acc[ct][1] = mfma(fr(af), pb[1][ks], acc[ct][1]);
        }
      }
#pragma unroll
      for (int ht = 0; ht < 2; ++ht)
#pragma unroll
        for (int T = 0; T < 2; ++T) {
          v4u o;
          o.x = nosent(cvtpk(acc[2 * T][ht][0], acc[2 * T][ht][1])); o.y = nosent(cvtpk(acc[2 * T][ht][2], acc[2 * T][ht][3]));
          o.z = nosent(cvtpk(acc[2 * T + 1][ht][0], acc[2 * T + 1][ht][1])); o.w = nosent(cvtpk(acc[2 * T + 1][ht][2], acc[2 * T + 1][ht][3]));
          ST128(o, WS_PART + ((16 * ht + l15) * 4 + (w >> 1)) * 32768 + (a * 128 + 64 * (w & 1) + 32 * T + 8 * g) * 2);
        }
    }
    STAMP(7);
    if (ffn) {
      const u8* b2 = p.wd + ((size_t)(fb * 64 + 16 * ks4 + 8 * (w >> 2) + 2 * g) * 512 + 128 * (w & 3) + 8 * l15) * 2;
#pragma unroll
      for (int s = 0; s < 2; ++s) f2[s] = ldnt(b2 + s * 1024);
      __builtin_amdgcn_sched_barrier(0);
    }
    wg_barrier();
    if (ffn) {
      const Sel sel = make_sel(l15);
      if (w < 4) {
        float v[8] = {bf_lo(xv.x), bf_hi(xv.x), bf_lo(xv.y), bf_hi(xv.y), bf_lo(xv.z), bf_hi(xv.z), bf_lo(xv.w), bf_hi(xv.w)};
        float s = 0.f;
#pragma unroll
        for (int i = 0; i < 8; ++i) s += v[i];
        const float mean = wave_sum(s) * (1.f / 512.f);
        float q2 = 0.f;
#pragma unroll
        for (int i = 0; i < 8; ++i) { v[i] -= mean; q2 += v[i] * v[i]; }
        const float rstd = __builtin_amdgcn_rsqf(wave_sum(q2) * (1.f / 512.f) + 1e-5f);
        v4u o;
        o.x = cvtpk(v[0] * rstd, v[1] * rstd); o.y = cvtpk(v[2] * rstd, v[3] * rstd);
        o.z = cvtpk(v[4] * rstd, v[5] * rstd); o.w = cvtpk(v[6] * rstd, v[7] * rstd);
        *(LAS v4u*)(L + AL_XN + w * 1040 + lane * 16) = o;
      }
      wg_barrier();
#pragma unroll
      for (int pp = 0; pp < 2; ++pp) {
        const v2u xs = *(LAS v2u*)(L + AL_XN + (2 * pp + (l15 >> 3)) * 1040 + (128 * ks4 + 16 * w + 4 * g) * 2);
        f32x4 ac = {0, 0, 0, 0};
#pragma unroll
        for (int s = 0; s < 4; ++s) ac = mfma(diagA(sel, xs[s >> 1], s & 1), f1[s], ac);
        *(LAS f32x4*)(L + AL_RED + ((w * 2 + pp) * 64 + lane) * 16) = ac;
      }
      wg_barrier();
      if (w < 2) {
        f32x4 s = *(LAS f32x4*)(L + AL_RED + ((0 * 2 + w) * 64 + lane) * 16);
#pragma unroll
        for (int ww = 1; ww < 8; ++ww) s += *(LAS f32x4*)(L + AL_RED + ((ww * 2 + w) * 64 + lane) * 16);
        const u32 rel = (2 * w + (g >> 1)) * 512 + (8 * l15 + 4 * (g & 1)) * 4;
        const v4u o = {cvtpk(s[0], s[1]), tag, cvtpk(s[2], s[3]), tag};
        ST128(o, WS_FP + a * 2048 + rel);
        const u32 off = WS_FP + (a & ~3) * 2048 + rel;
        float t[4];
        for (u32 spin = 0;; ++spin) {
          v4u v[4];
#pragma unroll
          for (int q = 0; q < 4; ++q) v[q] = LD128(off + q * 2048);
          bool ok = true;
          t[0] = t[1] = t[2] = t[3] = 0.f;
#pragma unroll
          for (int q = 0; q < 4; ++q) {
            ok &= (v[q].y == tag) & (v[q].w == tag);
            t[0] += bf_lo(v[q].x); t[1] += bf_hi(v[q].x); t[2] += bf_lo(v[q].z); t[3] += bf_hi(v[q].z);
          }
          if (__all(ok) || spin > SPIN_CAP) break;
          __builtin_amdgcn_s_sleep(2);
        }
        *(LAS f32x4*)(L + AL_FV + ((2 * w + (g >> 1)) * 128 + 8 * l15 + 4 * (g & 1)) * 4) = f32x4{t[0], t[1], t[2], t[3]};
      }
      wg_barrier();
      STAMP(11);
      if (w == 0) {
        const int bb = g, f = 16 * ks4 + l15;
        const float gv = *(LAS float*)(L + AL_FV + (bb * 128 + f) * 4), uv = *(LAS float*)(L + AL_FV + (bb * 128 + 64 + f) * 4);
        const float hv = gv * __builtin_amdgcn_rcpf(1.f + __expf(-gv)) * uv;
        *(LAS u16*)(L + AL_FH + bb * 64 + l15 * 2) = (u16)cvtpk(hv, 0.f);
      }
      wg_barrier();
      {
        f32x4 yy[2];
#pragma unroll
        for (int pp = 0; pp < 2; ++pp) {
          const u32 hs = *(LAS u32*)(L + AL_FH + (2 * pp + (l15 >> 3)) * 64 + (8 * (w >> 2) + 2 * g) * 2);
          f32x4 ac = {0, 0, 0, 0};
          ac = mfma(diagA(sel, hs, 0), f2[0], ac);
          ac = mfma(diagA(sel, hs, 1), f2[1], ac);
          yy[pp] = ac;
        }
        if (w >= 4) {
          *(LAS f32x4*)(L + AL_RED + (((w - 4) * 2 + 0) * 64 + lane) * 16) = yy[0];
          *(LAS f32x4*)(L + AL_RED + (((w - 4) * 2 + 1) * 64 + lane) * 16) = yy[1];
        }
        wg_barrier();
        if (w < 4) {
#pragma unroll
          for (int pp = 0; pp < 2; ++pp) {
            const f32x4 ac = yy[pp] + *(LAS f32x4*)(L + AL_RED + ((w * 2 + pp) * 64 + lane) * 16);
            const v4u o = {cvtpk(ac[0], ac[1]), tag, cvtpk(ac[2], ac[3]), tag};
            ST128(o, WS_YP + (((2 * pp + (g >> 1)) * 16 + 4 * w + (l15 >> 2)) * 112 + a) * 128 + (2 * (l15 & 3) + (g & 1)) * 16);
          }
        }
      }
    }
    STAMP(12);
    if ((a & 1) == 0) {
      const int fin = a >> 1;
      const int qd = tid & 7;
      const int o_ = fin * 32 + (tid & 31);
      const u32 xraw = *(const GAS u16*)(p.x + o_ * 2);
      v4u vs[2], vy[2];
      vy[1] = v4u{0, tag, 0, tag};
      for (u32 spin = 0;; ++spin) {
        vs[0] = LD128(WS_OUTP + fin * 16384 + tid * 16);
        vs[1] = LD128(WS_OUTP + fin * 16384 + 8192 + tid * 16);
        vy[0] = LD128(WS_YP + fin * 14336 + tid * 16);
        if (tid < 384) vy[1] = LD128(WS_YP + fin * 14336 + 8192 + tid * 16);
        const bool ok = (vs[0].y == tag) & (vs[0].w == tag) & (vs[1].y == tag) & (vs[1].w == tag) & (vy[0].y == tag) & (vy[0].w == tag) & (vy[1].y == tag) & (vy[1].w == tag);
        if (__all(ok) || spin > SPIN_CAP) break;
        __builtin_amdgcn_s_sleep(2);
      }
      STAMP(8);
      float r[8] = {bf_lo(vs[0].x) + bf_lo(vs[1].x), bf_hi(vs[0].x) + bf_hi(vs[1].x), bf_lo(vs[0].z) + bf_lo(vs[1].z), bf_hi(vs[0].z) + bf_hi(vs[1].z),
                    bf_lo(vy[0].x) + bf_lo(vy[1].x), bf_hi(vy[0].x) + bf_hi(vy[1].x), bf_lo(vy[0].z) + bf_lo(vy[1].z), bf_hi(vy[0].z) + bf_hi(vy[1].z)};
#pragma unroll
      for (int i = 0; i < 8; ++i) { float v = r[i]; v += dpp<0x128>(v); r[i] = xrow16_sum(v); }
      if (lane < 8) {
        *(LAS f32x4*)(L + AL_FR + (w * 2 + 0) * 128 + qd * 16) = f32x4{r[0], r[1], r[2], r[3]};
        *(LAS f32x4*)(L + AL_FR + (w * 2 + 1) * 128 + qd * 16) = f32x4{r[4], r[5], r[6], r[7]};
      }
      wg_barrier();
      if (tid < 32) {
        float s = 0.f, y = 0.f;
#pragma unroll
        for (int ww = 0; ww < 8; ++ww) {
          s += *(LAS float*)(L + AL_FR + (ww * 2 + 0) * 128 + tid * 4);
          y += *(LAS float*)(L + AL_FR + (ww * 2 + 1) * 128 + tid * 4);
        }
        const float gt = y * __builtin_amdgcn_rcpf(1.f + __expf(-y));
        *(GAS u16*)(p.out + o_ * 2) = (u16)cvtpk(bf_lo(xraw) + s * gt, 0.f);
      }
      if (a == 0 && tid == 0) ST32(tag, WS_EPOCH);
      STAMP(9);
    }
    STAMP_FLUSH();
  }
}

extern "C" void kernel_launch(void* const* d_in, const int* in_sizes, int n_in, void* d_out, int out_size, void* d_ws, size_t ws_size,
                              hipStream_t stream) {
  Params p;
  p.x = (const u8*)d_in[0]; p.ckv = (const u8*)d_in[1]; p.wq = (const u8*)d_in[2]; p.wuk = (const u8*)d_in[3];
  p.wuv = (const u8*)d_in[4]; p.wo = (const u8*)d_in[5]; p.wg = (const u8*)d_in[7]; p.wu = (const u8*)d_in[8]; p.wd = (const u8*)d_in[9];
  p.out = (u8*)d_out; p.ws = (u8*)d_ws;
  static bool attr = false;
  if (!attr) { (void)hipFuncSetAttribute((const void*)mla_ffn_kernel, hipFuncAttributeMaxDynamicSharedMemorySize, LDS_BYTES); attr = true; }
  hipLaunchKernelGGL(mla_ffn_kernel, dim3(NWG), dim3(NTHR), LDS_BYTES, stream, p);
}
```

```cpp
#include <hip/hip_runtime.h>
#include <stdint.h>

typedef unsigned char u8;
typedef unsigned short u16;
typedef unsigned int u32;
typedef unsigned long long u64;
typedef u32 v4u __attribute__((ext_vector_type(4)));
typedef u32 v2u __attribute__((ext_vector_type(2)));
typedef float f32x4 __attribute__((ext_vector_type(4)));
typedef float f32x16 __attribute__((ext_vector_type(16)));
typedef short bf16x8 __attribute__((ext_vector_type(8)));
typedef short s16x4 __attribute__((ext_vector_type(4)));
#define GAS __attribute__((address_space(1)))
#define LAS __attribute__((address_space(3)))
#define DI __device__ __forceinline__

constexpr int NWG = 256, NTHR = 512;
constexpr u32 SENT = 0xFFFFFFFFu;
constexpr int LDS_BYTES = 163840;

constexpr u32 WS_EPOCH = 0;
constexpr u32 WS_QF = 4096;
constexpr u32 WS_QA = 32768;
constexpr u32 WS_QP = WS_QA + 4 * 32768;
constexpr u32 WS_FP = WS_QP + 128 * 2048;
constexpr u32 WS_ST = WS_FP + 112 * 2048;
constexpr u32 WS_OUTP = WS_ST + 128 * 2048;
constexpr u32 WS_YP = WS_OUTP + 64 * 16384;
constexpr u32 WS_PART = WS_YP + 64 * 14336;
constexpr u32 WS_END = WS_PART + 128 * 32768;
constexpr u32 WS_STAMP = 200u << 20;

constexpr int AUX_SC1 = 16;
constexpr u32 SPIN_CAP = 1u << 17;
#define POLLSLEEP 2
#define ADELAY 30
#define CLRSLEEP 100
#define CKWIN 8

#define STAMPW(k, wv) do { } while (0)
#define STAMP_FLUSH() do { } while (0)
#define STAMP(k) STAMPW(k, 0)

struct Params {
  const u8* x; const u8* ckv; const u8* wq; const u8* wuk; const u8* wuv; const u8* wo;
  const u8* wg; const u8* wu; const u8* wd; u8* out; u8* ws;
};

DI float bf_lo(u32 v) { return __uint_as_float(v << 16); }
DI float bf_hi(u32 v) { return __uint_as_float(v & 0xFFFF0000u); }
DI u32 cvtpk(float lo, float hi) {
  typedef __bf16 bf2 __attribute__((ext_vector_type(2)));
  typedef float f2 __attribute__((ext_vector_type(2)));
  f2 f = {lo, hi};
  bf2 r = __builtin_convertvector(f, bf2);
  return __builtin_bit_cast(u32, r);
}
DI u32 nosent(u32 v) { return v < 0xFFFFFFFEu ? v : 0xFFFFFFFEu; }
DI v4u ldnt(const u8* p) { return __builtin_nontemporal_load((const GAS v4u*)p); }
DI f32x4 mfma(v4u a, v4u b, f32x4 c) {
  return __builtin_amdgcn_mfma_f32_16x16x32_bf16(__builtin_bit_cast(bf16x8, a), __builtin_bit_cast(bf16x8, b), c, 0, 0, 0);
}
DI u32 swz16(u32 k) { const u32 k2 = (k >> 2) & 1u, k3 = (k >> 3) & 1u; return k2 | ((k & 3u) << 1) | ((k2 ^ k3) << 3); }
DI void wg_barrier() { asm volatile("s_waitcnt lgkmcnt(0)\n\ts_barrier" ::: "memory"); }
DI void drain_vm() { asm volatile("s_waitcnt vmcnt(0)" ::: "memory"); }
DI float xrow16_max(float x) {
  auto s = __builtin_amdgcn_permlane16_swap(__float_as_uint(x), __float_as_uint(x), false, false);
  x = fmaxf(__uint_as_float(s[0]), __uint_as_float(s[1]));
  auto t = __builtin_amdgcn_permlane32_swap(__float_as_uint(x), __float_as_uint(x), false, false);
  return fmaxf(__uint_as_float(t[0]), __uint_as_float(t[1]));
}
DI float xrow16_sum(float x) {
  auto s = __builtin_amdgcn_permlane16_swap(__float_as_uint(x), __float_as_uint(x), false, false);
  x = __uint_as_float(s[0]) + __uint_as_float(s[1]);
  auto t = __builtin_amdgcn_permlane32_swap(__float_as_uint(x), __float_as_uint(x), false, false);
  return __uint_as_float(t[0]) + __uint_as_float(t[1]);
}
template <int CTRL> DI float dpp(float x) {
  return __builtin_bit_cast(float, __builtin_amdgcn_mov_dpp(__builtin_bit_cast(int, x), CTRL, 0xf, 0xf, true));
}
DI float row16_sum(float v) {
  v += dpp<0xB1>(v); v += dpp<0x4E>(v); v += dpp<0x124>(v); v += dpp<0x128>(v);
  return v;
}
DI float wave_sum(float v) { return xrow16_sum(row16_sum(v)); }
DI void team_bar(LAS u32* cnt, u32 target, int lane) {
  asm volatile("s_waitcnt lgkmcnt(0)" ::: "memory");
  if (lane == 0) __hip_atomic_fetch_add(cnt, 1u, __ATOMIC_RELAXED, __HIP_MEMORY_SCOPE_WORKGROUP);
  while (__hip_atomic_load(cnt, __ATOMIC_RELAXED, __HIP_MEMORY_SCOPE_WORKGROUP) < target) { }
  asm volatile("" ::: "memory");
}
struct Sel { u32 s[2][4]; };
DI Sel make_sel(int l15) {
  Sel r;
  const int jj = l15 & 7;
#pragma unroll
  for (int par = 0; par < 2; ++par) {
    const u32 hb = par ? 0x0302u : 0x0100u;
    const u32 v = (jj & 1) ? ((hb << 16) | 0x0C0Cu) : (0x0C0C0000u | hb);
#pragma unroll
    for (int k = 0; k < 4; ++k) r.s[par][k] = ((jj >> 1) == k) ? v : 0x0C0C0C0Cu;
  }
  return r;
}
DI v4u diagA(const Sel& sel, u32 src, int par) {
  v4u a;
  a.x = __builtin_amdgcn_perm(0u, src, sel.s[par][0]); a.y = __builtin_amdgcn_perm(0u, src, sel.s[par][1]);
  a.z = __builtin_amdgcn_perm(0u, src, sel.s[par][2]); a.w = __builtin_amdgcn_perm(0u, src, sel.s[par][3]);
  return a;
}

#define LD128(off) __builtin_amdgcn_raw_buffer_load_b128(rs, (off), 0, AUX_SC1)
#define LD64(off) __builtin_amdgcn_raw_buffer_load_b64(rs, (off), 0, AUX_SC1)
#define ST128(v, off) __builtin_amdgcn_raw_buffer_store_b128((v), rs, (off), 0, AUX_SC1)
#define ST64(v, off) __builtin_amdgcn_raw_buffer_store_b64((v), rs, (off), 0, AUX_SC1)
#define ST32(v, off) __builtin_amdgcn_raw_buffer_store_b32((v), rs, (off), 0, AUX_SC1)

struct TrB { u32 t0, t1; };
DI TrB tr_base(int lane) {
  const u32 g = lane >> 4, i = lane & 15, q = i >> 2, pp = i & 3;
  const u32 r0 = 8 * g + q, r1 = r0 + 4;
  TrB b;
  b.t0 = r0 * 256 + (((pp >> 1) ^ swz16(r0 & 15)) << 4) + 8 * (pp & 1);
  b.t1 = r1 * 256 + (((pp >> 1) ^ swz16(r1 & 15)) << 4) + 8 * (pp & 1);
  return b;
}
DI bf16x8 tr_frag256(LAS u8* L, const TrB& tb, u32 img_off  , u32 nt) {
  const s16x4 a = __builtin_amdgcn_ds_read_tr16_b64_v4i16((LAS s16x4*)(L + ((tb.t0 ^ (nt << 5)) + img_off)));
  const s16x4 b = __builtin_amdgcn_ds_read_tr16_b64_v4i16((LAS s16x4*)(L + ((tb.t1 ^ (nt << 5)) + img_off)));
  return __builtin_shufflevector(a, b, 0, 1, 2, 3, 4, 5, 6, 7);
}
DI void img256_write(LAS u8* img, int r0, int lane, v4u v) {
  const u32 row = (u32)(r0 + (lane >> 4));
  *(LAS v4u*)(img + row * 256 + (((u32)(lane & 15) ^ swz16(row & 15)) << 4)) = v;
}
DI v4u fr(bf16x8 x) { return __builtin_bit_cast(v4u, x); }
constexpr int WL_XN = 0;
constexpr int WL_Q = 4224;
constexpr int WL_CC = 5376;
constexpr int WL_CX = 6528;
constexpr int WL_IMG = 8192;
static_assert(WL_IMG + 131072 <= LDS_BYTES, "lds");
constexpr int AL_KV = 0;
constexpr int AL_Q = 131072;
constexpr int AL_MX = AL_Q;
constexpr int AL_LS = AL_Q + 1024;
constexpr int AL_P = AL_Q + 2048;
constexpr int AL_XN = 0;
constexpr int AL_RED = 8192;
constexpr int AL_FV = 24576;
constexpr int AL_FH = 26624;
constexpr int AL_FR = 28672;
constexpr int AL_CNT = 31744;
constexpr int AF_XN = AL_Q, AF_RED = AL_Q + 4352, AF_FV = AF_RED + 8192, AF_FH = AF_FV + 2048, AF_CNT = AF_FH + 512;
constexpr int AL_OT = 32768;

__global__ __launch_bounds__(NTHR, 2) void mla_ffn_kernel(Params p) {
  extern __shared__ __attribute__((aligned(16))) u8 smem[];
  LAS u8* L = (LAS u8*)smem;
  const int tid = threadIdx.x, lane = tid & 63;
  const int w = __builtin_amdgcn_readfirstlane(tid >> 6);
  const int bid = blockIdx.x, xcd = bid & 7, rr = bid >> 3;
  const __amdgpu_buffer_rsrc_t rs = __builtin_amdgcn_make_buffer_rsrc((void*)p.ws, 0, (int)WS_END, 0x00020000);
  const int g = lane >> 4, l15 = lane & 15;
  STAMP(0);
  const u64 t_start = __builtin_amdgcn_s_memrealtime();
  u32 ep_raw = __builtin_amdgcn_raw_buffer_load_b32(rs, WS_EPOCH, 0, 0);
#define GET_TAG() u32 tag; { u32 e_ = ep_raw; asm volatile("" : "+v"(e_)); tag = e_ + 1u; }
#define GET_TAG_DECL() u32 tag
#define GET_TAG_SET() { u32 e_ = ep_raw; asm volatile("" : "+v"(e_)); tag = e_ + 1u; }

#define WARM_LD(off) __builtin_amdgcn_raw_buffer_load_b32(rs, (off), 0, 0)
#define WARM_USE(x) asm volatile("" :: "v"(x))
  u32 wm0 = 0, wm1 = 0, wm2 = 0;
  if (rr < 16) {
    const int h = xcd * 4 + (rr >> 2), j = rr & 3, wi = h * 4 + j;
    const TrB trb = tr_base(lane);
    const int hf_ = lane >> 5;
    v4u xv = {0, 0, 0, 0};
    if (w < 4) xv = *(const GAS v4u*)(p.x + w * 1024 + lane * 16);
    if (w == 4) wm0 = WARM_LD(WS_QP + (wi & ~3) * 2048 + lane * 128);
    if (w == 5) {
      const v4u sv = {SENT, SENT, SENT, SENT};
      ST128(sv, WS_QA + (g * 32 + h) * 1024 + j * 256 + l15 * 16);
      wm0 = WARM_LD(WS_QF + (lane & 15) * 1024 + (wi >> 5) * 128);
    }
    v4u wqv[4];
#pragma unroll
    for (int i = 0; i < 4; ++i) wqv[i] = ldnt(p.wq + ((size_t)(128 * j + 16 * w + 4 * i + g) * 4096 + h * 128) * 2 + l15 * 16);
    __builtin_amdgcn_sched_barrier(0);
    v4u wuk[4];
    if (w < 4) {
      float v[8] = {bf_lo(xv.x), bf_hi(xv.x), bf_lo(xv.y), bf_hi(xv.y), bf_lo(xv.z), bf_hi(xv.z), bf_lo(xv.w), bf_hi(xv.w)};
      float s = 0.f;
#pragma unroll
      for (int i = 0; i < 8; ++i) s += v[i];
      const float mean = wave_sum(s) * (1.f / 512.f);
      float q2 = 0.f;
#pragma unroll
      for (int i = 0; i < 8; ++i) { v[i] -= mean; q2 += v[i] * v[i]; }
      const float rstd = __builtin_amdgcn_rsqf(wave_sum(q2) * (1.f / 512.f) + 1e-5f);
      v4u o;
      o.x = cvtpk(v[0] * rstd, v[1] * rstd); o.y = cvtpk(v[2] * rstd, v[3] * rstd);
      o.z = cvtpk(v[4] * rstd, v[5] * rstd); o.w = cvtpk(v[6] * rstd, v[7] * rstd);
      *(LAS v4u*)(L + WL_XN + w * 1040 + lane * 16) = o;
    }
#pragma unroll
    for (int i = 0; i < 4; ++i)
      wuk[i] = ldnt(p.wuk + ((size_t)(128 * j + 16 * w + 4 * i + g) * 4096 + h * 128) * 2 + l15 * 16);
    __builtin_amdgcn_sched_barrier(0);
#pragma unroll
    for (int i = 0; i < 4; ++i) img256_write(L + WL_IMG, 16 * w + 4 * i, lane, wqv[i]);
    wg_barrier();
    STAMP(1);
    GET_TAG();
    {
      f32x4 acc = {0, 0, 0, 0};
#pragma unroll
      for (int s = 0; s < 4; ++s) {
        const bf16x8 a = tr_frag256(L, trb, WL_IMG + s * 8192, w);
        const v4u b = *(LAS v4u*)(L + WL_XN + (l15 & 3) * 1040 + (128 * j + 32 * s + 8 * g) * 2);
        acc = mfma(fr(a), b, acc);
      }
      if (l15 < 4) {
        const v2u o = {cvtpk(acc[0], acc[1]), cvtpk(acc[2], acc[3])};
        *(LAS v2u*)(L + WL_CC + l15 * 272 + (16 * w + 4 * g) * 2) = o;
      }
      wg_barrier();
      if (w < 2) {
        const u32 rel = (2 * w + (lane >> 5)) * 512 + (lane & 31) * 16;
        {
          const v2u o = *(LAS v2u*)(L + WL_CC + (2 * w + (lane >> 5)) * 272 + (lane & 31) * 8);
          const v4u o4 = {o.x, tag, o.y, tag};
          ST128(o4, WS_QP + wi * 2048 + rel);
        }
        float t[4];
        for (u32 spin = 0;; ++spin) {
          v4u v[4];
#pragma unroll
          for (int q = 0; q < 4; ++q) v[q] = LD128(WS_QP + (h * 4 + q) * 2048 + rel);
          bool ok = true;
          t[0] = t[1] = t[2] = t[3] = 0.f;
#pragma unroll
          for (int q = 0; q < 4; ++q) {
            ok &= (v[q].y == tag) & (v[q].w == tag);
            t[0] += bf_lo(v[q].x); t[1] += bf_hi(v[q].x); t[2] += bf_lo(v[q].z); t[3] += bf_hi(v[q].z);
          }
          if (__all(ok) || spin > SPIN_CAP) break;
          __builtin_amdgcn_s_sleep(1);
        }
        const v2u o2 = {cvtpk(t[0], t[1]), cvtpk(t[2], t[3])};
        *(LAS v2u*)(L + WL_Q + (2 * w + (lane >> 5)) * 272 + (lane & 31) * 8) = o2;
      }
    }
    v4u ukf[4];
    {
      LAS u8* im = L + WL_IMG + 32768 + w * 4096;
#pragma unroll
      for (int i = 0; i < 4; ++i) img256_write(im, 4 * i, lane, wuk[i]);
#pragma unroll
      for (int s = 0; s < 4; ++s) ukf[s] = *(LAS v4u*)(im + l15 * 256 + (((u32)(4 * s + g) ^ swz16(l15)) << 4));
    }
    WARM_USE(wm0);
    if (w == 5) drain_vm();
    wg_barrier();
    STAMP(2);
    {
      f32x4 acc = {0, 0, 0, 0};
#pragma unroll
      for (int s = 0; s < 4; ++s) acc = mfma(ukf[s], *(LAS v4u*)(L + WL_Q + (l15 & 3) * 272 + (32 * s + 8 * g) * 2), acc);
      constexpr float SC = 0.08838834764831845f * 1.4426950408889634f;
      if (l15 < 4) {
        const v2u o = {cvtpk(acc[0] * SC, acc[1] * SC), cvtpk(acc[2] * SC, acc[3] * SC)};
        *(LAS v2u*)(L + WL_CX + l15 * 272 + (16 * w + 4 * g) * 2) = o;
      }
    }
    wg_barrier();
    if (w == 0) {
      v4u o = *(LAS v4u*)(L + WL_CX + g * 272 + l15 * 16);
      o.x = nosent(o.x); o.y = nosent(o.y); o.z = nosent(o.z); o.w = nosent(o.w);
      ST128(o, WS_QA + (g * 32 + h) * 1024 + j * 256 + l15 * 16);
      if (lane < 16) ST32(tag, WS_QF + lane * 1024 + wi * 4);
    }
    STAMP(3);
    if (w == 6) { wm1 = WARM_LD(lane < 16 ? WS_ST + wi * 2048 + lane * 128 : WS_FP + (wi < 112 ? wi : 0) * 2048 + (lane & 15) * 128); }
    if (w == 7) { wm1 = WARM_LD(WS_OUTP + wi * 8192 + lane * 128); wm2 = WARM_LD(WS_YP + wi * 7168 + (lane < 56 ? lane : 0) * 128); }
    bf16x8 fuv[4], fo[4][4];
    {
      v4u t[4];
#pragma unroll
      for (int i = 0; i < 4; ++i)
        t[i] = ldnt(p.wuv + ((size_t)(128 * j + 16 * w + 4 * i + g) * 4096 + h * 128) * 2 + l15 * 16);
      v4u u[4][4];
#define WO_ISSUE(ss) _Pragma("unroll") for (int i = 0; i < 4; ++i) u[ss][i] = ldnt(p.wo + ((size_t)(h * 128 + 32 * (((ss) + j) & 3) + 4 * w + i) * 512) * 2 + lane * 16); __builtin_amdgcn_sched_barrier(0);
      WO_ISSUE(0)
#pragma unroll
      for (int i = 0; i < 4; ++i) img256_write(L + WL_IMG, 16 * w + 4 * i, lane, t[i]);
      wg_barrier();
#pragma unroll
      for (int s = 0; s < 4; ++s) fuv[s] = tr_frag256(L, trb, WL_IMG + s * 8192, w);
#pragma unroll
      for (int ss = 0; ss < 4; ++ss) {
        if (ss == 0) { WO_ISSUE(1) }
        if (ss == 1) { WO_ISSUE(2) }
        if (ss == 2) { WO_ISSUE(3) }
        LAS u8* img = L + WL_IMG + ((ss + 1) & 3) * 32768;
        if (ss == 3) wg_barrier();
#pragma unroll
        for (int i = 0; i < 4; ++i) {
          const u32 row = 4 * w + i;
          *(LAS v4u*)(img + g * 8192 + row * 256 + (((u32)l15 ^ swz16(row & 15)) << 4)) = u[ss][i];
        }
        wg_barrier();
#pragma unroll
        for (int n = 0; n < 4; ++n) fo[ss][n] = tr_frag256(L, trb, WL_IMG + ((ss + 1) & 3) * 32768 + (w >> 1) * 8192, 4 * (w & 1) + n);
      }
    }
    WARM_USE(wm1); WARM_USE(wm2);
    STAMP(4);
    if (w < 4) {
      const int b = w;
      u32 asc[8];
      float invL;
      {
        float m[8], lsum[8];
        for (u32 spin = 0;; ++spin) {
          bool ok = true;
#pragma unroll
          for (int s = 0; s < 8; ++s) {
            const v4u v = LD128(WS_ST + wi * 2048 + (b * 32 + 4 * s + g) * 16);
            ok &= (v.y == tag) & (v.w == tag);
            m[s] = __uint_as_float(v.x); lsum[s] = __uint_as_float(v.z);
          }
          if (__all(ok) || spin > SPIN_CAP) break;
          __builtin_amdgcn_s_sleep(4);
        }
        float M = m[0];
#pragma unroll
        for (int s = 1; s < 8; ++s) M = fmaxf(M, m[s]);
        M = xrow16_max(M);
        float Ls = 0.f;
#pragma unroll
        for (int s = 0; s < 8; ++s) {
          const u32 e = cvtpk(__builtin_amdgcn_exp2f(m[s] - M), 0.f) & 0xFFFFu;
          asc[s] = e;
          Ls += bf_lo(e) * lsum[s];
        }
        Ls = xrow16_sum(Ls);
        invL = __builtin_amdgcn_rcpf(Ls);
      }
      __builtin_amdgcn_fence(__ATOMIC_ACQUIRE, "wavefront");
      STAMP(5);
      v4u v[8];
      for (u32 spin = 0;; ++spin) {
        bool ok = true;
#pragma unroll
        for (int s = 0; s < 8; ++s) {
          v[s] = LD128(WS_PART + wi * 32768 + ((b * 32 + 4 * s + g) * 128) * 2 + l15 * 16);
          ok &= (v[s].x != SENT) & (v[s].y != SENT) & (v[s].z != SENT) & (v[s].w != SENT);
        }
        if (__all(ok) || spin > SPIN_CAP) break;
        __builtin_amdgcn_s_sleep(1);
      }
      STAMP(6);
      f32x4 acc = {0, 0, 0, 0};
#pragma unroll
      for (int s = 0; s < 8; ++s) {
        v4u a = diagA(make_sel(l15), asc[s], 0);
        if (l15 >= 8) a = v4u{0, 0, 0, 0};
        acc = mfma(a, v[s], acc);
      }
      if (g < 2) {
        const v2u o = {cvtpk(acc[0] * invL, acc[1] * invL), cvtpk(acc[2] * invL, acc[3] * invL)};
        *(LAS v2u*)(L + WL_CC + b * 272 + (32 * (l15 >> 2) + 16 * g + 4 * (l15 & 3)) * 2) = o;
      }
    }
    wg_barrier();
    {
      f32x4 acc = {0, 0, 0, 0};
#pragma unroll
      for (int s = 0; s < 4; ++s) acc = mfma(fr(fuv[s]), *(LAS v4u*)(L + WL_CC + (l15 & 3) * 272 + (32 * s + 8 * g) * 2), acc);
      if (l15 < 4) {
        const v2u o = {cvtpk(acc[0], acc[1]), cvtpk(acc[2], acc[3])};
        *(LAS v2u*)(L + WL_CX + l15 * 272 + (16 * w + 4 * g) * 2) = o;
      }
    }
    wg_barrier();
    {
      v4u bb[4];
#pragma unroll
      for (int ss = 0; ss < 4; ++ss) bb[ss] = *(LAS v4u*)(L + WL_CX + (l15 & 3) * 272 + (32 * ((ss + j) & 3) + 8 * g) * 2);
      f32x4 acc[4];
#pragma unroll
      for (int n = 0; n < 4; ++n) {
        acc[n] = f32x4{0, 0, 0, 0};
#pragma unroll
        for (int ss = 0; ss < 4; ++ss) acc[n] = mfma(fr(fo[ss][n]), bb[ss], acc[n]);
      }
      const int u = l15 >> 2;
      const f32x4 a = u == 0 ? acc[0] : (u == 1 ? acc[1] : (u == 2 ? acc[2] : acc[3]));
      const v4u o = {cvtpk(a[0], a[1]), tag, cvtpk(a[2], a[3]), tag};
      ST128(o, WS_OUTP + (((l15 & 3) * 16 + 2 * w + (u >> 1)) * 128 + wi) * 128 + ((u & 1) * 4 + g) * 16);
    }
    STAMP(7);
    STAMP_FLUSH();
  } else {
    const int a = xcd * 16 + (rr - 16), b = a >> 5, ch = a & 31;
    const int fb = a >> 2, ks4 = a & 3;
    const bool ffn = fb < 28;
    v4u xv = {0, 0, 0, 0};
    const int l31 = lane & 31, hf = lane >> 5;
    GET_TAG_DECL();
    if (w < 4) {
      v4u fa[32];
      {
        constexpr int WIN = CKWIN;
        const u8* src = p.ckv + ((size_t)(b * 4096 + ch * 128 + 32 * w) * 512) * 2 + lane * 16;
        __builtin_amdgcn_s_sleep(ADELAY);
        v4u r[WIN];
#pragma unroll
        for (int i = 0; i < WIN; ++i) r[i] = ldnt(src + i * 1024);
        __builtin_amdgcn_sched_barrier(0);
        if (tid == 0) *(LAS u32*)(L + AF_CNT) = 0;
        wg_barrier();
#pragma unroll
        for (int i = 0; i < 32; ++i) {
          const v4u v = r[i % WIN];
          *(LAS v4u*)(L + (((u32)(AL_KV + 32 * w * 1024 + lane * 16) ^ (swz16(i & 15) << 4)) + (u32)(i * 1024))) = v;
          if (i + WIN < 32) r[i % WIN] = ldnt(src + (i + WIN) * 1024);
          __builtin_amdgcn_sched_barrier(0);
        }
        STAMP(1);
      }
      const u32 fsw = ((u32)hf ^ swz16(l15)) << 4;
      {
        const u32 a0 = (u32)(AL_KV + (32 * w + l31) * 1024) + fsw;
#pragma unroll
        for (int s = 0; s < 32; ++s) fa[s] = *(LAS v4u*)(L + ((a0 ^ (u32)((s & 7) << 5)) + (u32)((s >> 3) * 256)));
      }
      wg_barrier();
      STAMP(4);
      f32x16 S, S2;
#pragma unroll
      for (int i = 0; i < 16; ++i) { S[i] = 0.f; S2[i] = 0.f; }
      {
        v4u bq[2][8];
        const u32 q0 = (u32)(AL_Q + l31 * 1024) + fsw;
#define BQ_LOAD(grp) _Pragma("unroll") for (int i = 0; i < 8; ++i) bq[(grp) & 1][i] = *(LAS v4u*)(L + ((q0 ^ (u32)(i << 5)) + (u32)((grp) * 256)));
        BQ_LOAD(0)
#pragma unroll
        for (int gq = 0; gq < 4; ++gq) {
          if (gq < 3) { BQ_LOAD(gq + 1) }
          __builtin_amdgcn_sched_barrier(0);
#pragma unroll
          for (int i = 0; i < 8; i += 2) {
            S = __builtin_amdgcn_mfma_f32_32x32x16_bf16(__builtin_bit_cast(bf16x8, fa[8 * gq + i]), __builtin_bit_cast(bf16x8, bq[gq & 1][i]), S, 0, 0, 0);
            S2 = __builtin_amdgcn_mfma_f32_32x32x16_bf16(__builtin_bit_cast(bf16x8, fa[8 * gq + i + 1]), __builtin_bit_cast(bf16x8, bq[gq & 1][i + 1]), S2, 0, 0, 0);
          }
          __builtin_amdgcn_sched_barrier(0);
        }
      }
      S += S2;
      STAMP(15);
      float mt = S[0];
#pragma unroll
      for (int i = 1; i < 16; ++i) mt = fmaxf(mt, S[i]);
      {
        auto t = __builtin_amdgcn_permlane32_swap(__float_as_uint(mt), __float_as_uint(mt), false, false);
        mt = fmaxf(__uint_as_float(t[0]), __uint_as_float(t[1]));
      }
      STAMP(14);
      wg_barrier();
      if (hf == 0) *(LAS float*)(L + AL_MX + (l31 * 4 + w) * 4) = mt;
      if (tid == 0) *(LAS u32*)(L + AL_CNT) = 0;
      wg_barrier();
      STAMP(5);
      const f32x4 m4 = *(LAS f32x4*)(L + AL_MX + l31 * 16);
      const float M = fmaxf(fmaxf(m4[0], m4[1]), fmaxf(m4[2], m4[3]));
      u32 pk[8];
      float ls = 0.f;
#pragma unroll
      for (int i = 0; i < 8; ++i) {
        pk[i] = cvtpk(__builtin_amdgcn_exp2f(S[2 * i] - M), __builtin_amdgcn_exp2f(S[2 * i + 1] - M));
        ls += bf_lo(pk[i]) + bf_hi(pk[i]);
      }
      {
        auto t = __builtin_amdgcn_permlane32_swap(__float_as_uint(ls), __float_as_uint(ls), false, false);
        ls = __uint_as_float(t[0]) + __uint_as_float(t[1]);
      }
#pragma unroll
      for (int u = 0; u < 2; ++u) {
        const v4u o = {pk[4 * u], pk[4 * u + 1], pk[4 * u + 2], pk[4 * u + 3]};
        *(LAS v4u*)(L + AL_P + (((w * 2 + (l31 >> 4)) * 64) + (2 * u + hf) * 16 + l15) * 16) = o;
      }
      if (hf == 0) *(LAS float*)(L + AL_LS + (l31 * 4 + w) * 4) = ls;
      wg_barrier();
      STAMP(6);
      GET_TAG_SET();
      if (w == 3) {
        const f32x4 l4 = *(LAS f32x4*)(L + AL_LS + l31 * 16);
        const float Ls = (l4[0] + l4[1]) + (l4[2] + l4[3]);
        const v4u o = {__float_as_uint(M), tag, __float_as_uint(Ls), tag};
        ST128(o, WS_ST + (l31 * 4 + hf * 2) * 2048 + a * 16);
        ST128(o, WS_ST + (l31 * 4 + hf * 2 + 1) * 2048 + a * 16);
      }
    } else {
      const int w4 = w - 4;
      if (ffn) {
        LAS u32* tb = (LAS u32*)(L + AF_CNT);
        const Sel sel = make_sel(l15);
        const v4u xv4 = *(const GAS v4u*)(p.x + w4 * 1024 + lane * 16);
        v4u f1[8], f2[4];
        {
          const u8* b1 = (l15 < 8 ? p.wg : p.wu) + ((size_t)(128 * ks4 + 32 * w4 + 8 * g) * 1792 + fb * 64 + (l15 & 7) * 8) * 2;
#pragma unroll
          for (int s = 0; s < 8; ++s) f1[s] = ldnt(b1 + s * 3584);
          __builtin_amdgcn_sched_barrier(0);
        }
        {
          float v[8] = {bf_lo(xv4.x), bf_hi(xv4.x), bf_lo(xv4.y), bf_hi(xv4.y), bf_lo(xv4.z), bf_hi(xv4.z), bf_lo(xv4.w), bf_hi(xv4.w)};
          float s = 0.f;
#pragma unroll
          for (int i = 0; i < 8; ++i) s += v[i];
          const float mean = wave_sum(s) * (1.f / 512.f);
          float q2 = 0.f;
#pragma unroll
          for (int i = 0; i < 8; ++i) { v[i] -= mean; q2 += v[i] * v[i]; }
          const float rstd = __builtin_amdgcn_rsqf(wave_sum(q2) * (1.f / 512.f) + 1e-5f);
          v4u o;
          o.x = cvtpk(v[0] * rstd, v[1] * rstd); o.y = cvtpk(v[2] * rstd, v[3] * rstd);
          o.z = cvtpk(v[4] * rstd, v[5] * rstd); o.w = cvtpk(v[6] * rstd, v[7] * rstd);
          *(LAS v4u*)(L + AF_XN + w4 * 1040 + lane * 16) = o;
        }
        {
          const u8* b2 = p.wd + ((size_t)(fb * 64 + 16 * ks4 + 4 * g) * 512 + 128 * w4 + 8 * l15) * 2;
#pragma unroll
          for (int s = 0; s < 4; ++s) f2[s] = ldnt(b2 + s * 1024);
          __builtin_amdgcn_sched_barrier(0);
        }
        wg_barrier();
        GET_TAG_SET();
#pragma unroll
        for (int pp = 0; pp < 2; ++pp) {
          const v4u xs = *(LAS v4u*)(L + AF_XN + (2 * pp + (l15 >> 3)) * 1040 + (128 * ks4 + 32 * w4 + 8 * g) * 2);
          f32x4 ac = {0, 0, 0, 0};
#pragma unroll
          for (int s = 0; s < 8; ++s) ac = mfma(diagA(sel, xs[s >> 1], s & 1), f1[s], ac);
          *(LAS f32x4*)(L + AF_RED + ((w4 * 2 + pp) * 64 + lane) * 16) = ac;
        }
        team_bar(tb, 4, lane);
        if (w4 < 2) {
          f32x4 s = *(LAS f32x4*)(L + AF_RED + ((0 * 2 + w4) * 64 + lane) * 16);
#pragma unroll
          for (int ww = 1; ww < 4; ++ww) s += *(LAS f32x4*)(L + AF_RED + ((ww * 2 + w4) * 64 + lane) * 16);
          const u32 rel = (2 * w4 + (g >> 1)) * 512 + (8 * l15 + 4 * (g & 1)) * 4;
          const v4u o = {cvtpk(s[0], s[1]), tag, cvtpk(s[2], s[3]), tag};
          ST128(o, WS_FP + a * 2048 + rel);
          const u32 off = WS_FP + (a & ~3) * 2048 + rel;
          float t[4];
          for (u32 spin = 0;; ++spin) {
            v4u v[4];
#pragma unroll
            for (int q = 0; q < 4; ++q) v[q] = LD128(off + q * 2048);
            bool ok = true;
            t[0] = t[1] = t[2] = t[3] = 0.f;
#pragma unroll
            for (int q = 0; q < 4; ++q) {
              ok &= (v[q].y == tag) & (v[q].w == tag);
              t[0] += bf_lo(v[q].x); t[1] += bf_hi(v[q].x); t[2] += bf_lo(v[q].z); t[3] += bf_hi(v[q].z);
            }
            if (__all(ok) || spin > SPIN_CAP) break;
            __builtin_amdgcn_s_sleep(4);
          }
          *(LAS f32x4*)(L + AF_FV + ((2 * w4 + (g >> 1)) * 128 + 8 * l15 + 4 * (g & 1)) * 4) = f32x4{t[0], t[1], t[2], t[3]};
        }
        team_bar(tb, 8, lane);
        STAMPW(11, 4);
        if (w4 == 0) {
          const int bb = g, f = 16 * ks4 + l15;
          const float gv = *(LAS float*)(L + AF_FV + (bb * 128 + f) * 4), uv = *(LAS float*)(L + AF_FV + (bb * 128 + 64 + f) * 4);
          const float hv = gv * __builtin_amdgcn_rcpf(1.f + __expf(-gv)) * uv;
          *(LAS u16*)(L + AF_FH + bb * 64 + l15 * 2) = (u16)cvtpk(hv, 0.f);
        }
        team_bar(tb, 12, lane);
#pragma unroll
        for (int pp = 0; pp < 2; ++pp) {
          const v2u hs = *(LAS v2u*)(L + AF_FH + (2 * pp + (l15 >> 3)) * 64 + (4 * g) * 2);
          f32x4 ac = {0, 0, 0, 0};
#pragma unroll
          for (int s = 0; s < 4; ++s) ac = mfma(diagA(sel, hs[s >> 1], s & 1), f2[s], ac);
          const v4u o = {cvtpk(ac[0], ac[1]), tag, cvtpk(ac[2], ac[3]), tag};
          ST128(o, WS_YP + (((2 * pp + (g >> 1)) * 16 + 4 * w4 + (l15 >> 2)) * 112 + a) * 128 + (2 * (l15 & 3) + (g & 1)) * 16);
        }
        team_bar(tb, 16, lane);
        STAMPW(12, 4);
      } else {
        wg_barrier();
      }
      GET_TAG_SET();
      if (w >= 6) {
        const int h0 = 16 * (w - 6);
        for (u32 spin = 0;; ++spin) {
          const u32 f = __builtin_amdgcn_raw_buffer_load_b32(rs, WS_QF + (a & 15) * 1024 + h0 * 16 + lane * 4, 0, AUX_SC1);
          if (__all(f == tag) || spin > SPIN_CAP) break;
          __builtin_amdgcn_s_sleep(POLLSLEEP);
        }
        __builtin_amdgcn_fence(__ATOMIC_ACQUIRE, "wavefront");
        STAMPW(2, 6);
        for (u32 spin = 0;; ++spin) {
          v4u t[16];
#pragma unroll
          for (int i = 0; i < 16; ++i) t[i] = LD128(WS_QA + (b * 32 + h0 + i) * 1024 + lane * 16);
          bool ok = true;
#pragma unroll
          for (int i = 0; i < 16; ++i) {
            ok &= (t[i].x != SENT) & (t[i].y != SENT) & (t[i].z != SENT) & (t[i].w != SENT);
            *(LAS v4u*)(L + (((u32)(AL_Q + lane * 16) ^ (swz16(i) << 4)) + (u32)((h0 + i) * 1024))) = t[i];
          }
          if (__all(ok) || spin > SPIN_CAP) break;
          __builtin_amdgcn_s_sleep(2);
        }
        STAMPW(3, 6);
      }
      wg_barrier();
      if (w == 5) {
        const v4u sv = {SENT, SENT, SENT, SENT};
#pragma unroll
        for (int i = 0; i < 32; ++i) ST128(sv, WS_PART + (4 * i + g) * 32768 + a * 256 + l15 * 16);
      }
      v4u ft[32];
      {
        const int q = l15 >> 2, pp = l15 & 3, hf2 = g & 1, u2 = g >> 1;
        const u32 r0 = 16 * u2 + 4 * hf2 + q;
        const u32 t0 = (u32)AL_KV + r0 * 1024 + w4 * 256 + ((((u32)pp >> 1) ^ swz16(r0 & 15)) << 4) + 8 * (pp & 1);
        const u32 t1 = (u32)AL_KV + (r0 + 8) * 1024 + w4 * 256 + ((((u32)pp >> 1) ^ swz16((r0 + 8) & 15)) << 4) + 8 * (pp & 1);
#pragma unroll
        for (int ct = 0; ct < 8; ++ct) {
#pragma unroll
          for (int kh = 0; kh < 2; ++kh) {
            const u32 b0 = (t0 ^ (u32)(ct << 5)) + (u32)(kh * 65536), b1 = (t1 ^ (u32)(ct << 5)) + (u32)(kh * 65536);
#pragma unroll
            for (int k2 = 0; k2 < 2; ++k2) {
              const s16x4 x0 = __builtin_amdgcn_ds_read_tr16_b64_v4i16((LAS s16x4*)(L + b0 + k2 * 32768));
              const s16x4 x1 = __builtin_amdgcn_ds_read_tr16_b64_v4i16((LAS s16x4*)(L + b1 + k2 * 32768));
              ft[ct * 4 + 2 * kh + k2] = __builtin_bit_cast(v4u, __builtin_shufflevector(x0, x1, 0, 1, 2, 3, 4, 5, 6, 7));
            }
          }
        }
      }
      STAMPW(13, 4);
      wg_barrier();
      wg_barrier();
      if (w == 5) { drain_vm(); STAMPW(10, 5); }
      wg_barrier();
      v4u pb[2][4];
#pragma unroll
      for (int kt = 0; kt < 4; ++kt)
#pragma unroll
        for (int ht = 0; ht < 2; ++ht) pb[ht][kt] = *(LAS v4u*)(L + AL_P + ((kt * 2 + ht) * 64 + lane) * 16);
#pragma unroll
      for (int T = 0; T < 4; ++T) {
        f32x4 acc[2][2];
#pragma unroll
        for (int c2 = 0; c2 < 2; ++c2)
#pragma unroll
          for (int ht = 0; ht < 2; ++ht) {
            f32x4 z = {0, 0, 0, 0};
#pragma unroll
            for (int kt = 0; kt < 4; ++kt) z = mfma(ft[(2 * T + c2) * 4 + kt], pb[ht][kt], z);
            acc[c2][ht] = z;
          }
#pragma unroll
        for (int ht = 0; ht < 2; ++ht) {
          v4u o;
          o.x = nosent(cvtpk(acc[0][ht][0], acc[0][ht][1])); o.y = nosent(cvtpk(acc[0][ht][2], acc[0][ht][3]));
          o.z = nosent(cvtpk(acc[1][ht][0], acc[1][ht][1])); o.w = nosent(cvtpk(acc[1][ht][2], acc[1][ht][3]));
          *(LAS v4u*)(L + AL_OT + w4 * 8192 + (16 * ht + l15) * 256 + (((u32)(4 * T + g) ^ (u32)l15) << 4)) = o;
        }
      }
#pragma unroll
      for (int i = 0; i < 8; ++i) {
        const u32 hd = 4 * i + g;
        const v4u o = *(LAS v4u*)(L + AL_OT + w4 * 8192 + hd * 256 + (((u32)l15 ^ (hd & 15)) << 4));
        ST128(o, WS_PART + (hd * 4 + w4) * 32768 + a * 256 + l15 * 16);
      }
      STAMPW(7, 4);
    }
    wg_barrier();
    if ((a & 1) == 0) {
      const int fin = a >> 1;
      const int qd = tid & 7;
      const int o_ = fin * 32 + (tid & 31);
      const u32 xraw = *(const GAS u16*)(p.x + o_ * 2);
      v4u vs[2], vy[2];
      vy[1] = v4u{0, tag, 0, tag};
      for (u32 spin = 0;; ++spin) {
        vs[0] = LD128(WS_OUTP + fin * 16384 + tid * 16);
        vs[1] = LD128(WS_OUTP + fin * 16384 + 8192 + tid * 16);
        vy[0] = LD128(WS_YP + fin * 14336 + tid * 16);
        if (tid < 384) vy[1] = LD128(WS_YP + fin * 14336 + 8192 + tid * 16);
        const bool ok = (vs[0].y == tag) & (vs[0].w == tag) & (vs[1].y == tag) & (vs[1].w == tag) & (vy[0].y == tag) & (vy[0].w == tag) & (vy[1].y == tag) & (vy[1].w == tag);
        if (__all(ok) || spin > SPIN_CAP) break;
        __builtin_amdgcn_s_sleep(2);
      }
      STAMP(8);
      float r[8] = {bf_lo(vs[0].x) + bf_lo(vs[1].x), bf_hi(vs[0].x) + bf_hi(vs[1].x), bf_lo(vs[0].z) + bf_lo(vs[1].z), bf_hi(vs[0].z) + bf_hi(vs[1].z),
                    bf_lo(vy[0].x) + bf_lo(vy[1].x), bf_hi(vy[0].x) + bf_hi(vy[1].x), bf_lo(vy[0].z) + bf_lo(vy[1].z), bf_hi(vy[0].z) + bf_hi(vy[1].z)};
#pragma unroll
      for (int i = 0; i < 8; ++i) { float v = r[i]; v += dpp<0x128>(v); r[i] = xrow16_sum(v); }
      if (lane < 8) {
        *(LAS f32x4*)(L + AL_FR + (w * 2 + 0) * 128 + qd * 16) = f32x4{r[0], r[1], r[2], r[3]};
        *(LAS f32x4*)(L + AL_FR + (w * 2 + 1) * 128 + qd * 16) = f32x4{r[4], r[5], r[6], r[7]};
      }
      wg_barrier();
      if (tid < 32) {
        float s = 0.f, y = 0.f;
#pragma unroll
        for (int ww = 0; ww < 8; ++ww) {
          s += *(LAS float*)(L + AL_FR + (ww * 2 + 0) * 128 + tid * 4);
          y += *(LAS float*)(L + AL_FR + (ww * 2 + 1) * 128 + tid * 4);
        }
        const float gt = y * __builtin_amdgcn_rcpf(1.f + __expf(-y));
        *(GAS u16*)(p.out + o_ * 2) = (u16)cvtpk(bf_lo(xraw) + s * gt, 0.f);
      }
      if (a == 0 && tid == 0) ST32(tag, WS_EPOCH);
      STAMP(9);
    }
    STAMP_FLUSH();
  }
}

extern "C" void kernel_launch(void* const* d_in, const int* in_sizes, int n_in, void* d_out, int out_size, void* d_ws, size_t ws_size,
                              hipStream_t stream) {
  Params p;
  p.x = (const u8*)d_in[0]; p.ckv = (const u8*)d_in[1]; p.wq = (const u8*)d_in[2]; p.wuk = (const u8*)d_in[3];
  p.wuv = (const u8*)d_in[4]; p.wo = (const u8*)d_in[5]; p.wg = (const u8*)d_in[7]; p.wu = (const u8*)d_in[8]; p.wd = (const u8*)d_in[9];
  p.out = (u8*)d_out; p.ws = (u8*)d_ws;
  static bool attr = false;
  if (!attr) { (void)hipFuncSetAttribute((const void*)mla_ffn_kernel, hipFuncAttributeMaxDynamicSharedMemorySize, LDS_BYTES); attr = true; }
  hipLaunchKernelGGL(mla_ffn_kernel, dim3(NWG), dim3(NTHR), LDS_BYTES, stream, p);
}
```

```cpp
#include <hip/hip_runtime.h>
#include <stdint.h>

typedef unsigned char u8;
typedef unsigned short u16;
typedef unsigned int u32;
typedef unsigned long long u64;
typedef u32 v4u __attribute__((ext_vector_type(4)));
typedef u32 v2u __attribute__((ext_vector_type(2)));
typedef float f32x4 __attribute__((ext_vector_type(4)));
typedef float f32x16 __attribute__((ext_vector_type(16)));
typedef short bf16x8 __attribute__((ext_vector_type(8)));
typedef short s16x4 __attribute__((ext_vector_type(4)));
#define GAS __attribute__((address_space(1)))
#define LAS __attribute__((address_space(3)))
#define DI __device__ __forceinline__

constexpr int NWG = 256, NTHR = 512;
constexpr u32 SENT = 0xFFFFFFFFu;
constexpr int LDS_BYTES = 163840;

constexpr u32 WS_EPOCH = 0;
constexpr u32 WS_QF = 4096;
constexpr u32 WS_QA = 32768;
constexpr u32 WS_QP = WS_QA + 4 * 32768;
constexpr u32 WS_FP = WS_QP + 128 * 2048;
constexpr u32 WS_ST = WS_FP + 112 * 2048;
constexpr u32 WS_OUTP = WS_ST + 128 * 2048;
constexpr u32 WS_YP = WS_OUTP + 64 * 16384;
constexpr u32 WS_PART = WS_YP + 64 * 14336;
constexpr u32 WS_END = WS_PART + 128 * 32768;
constexpr u32 WS_STAMP = 200u << 20;

constexpr int AUX_SC1 = 16;
constexpr u32 SPIN_CAP = 1u << 17;
#define POLLSLEEP 2

#define CLRSLEEP 100
#define CKWIN 10

#define STAMPW(k, wv) do { } while (0)
#define STAMP_FLUSH() do { } while (0)
#define STAMP(k) STAMPW(k, 0)

struct Params {
  const u8* x; const u8* ckv; const u8* wq; const u8* wuk; const u8* wuv; const u8* wo;
  const u8* wg; const u8* wu; const u8* wd; u8* out; u8* ws;
};

DI float bf_lo(u32 v) { return __uint_as_float(v << 16); }
DI float bf_hi(u32 v) { return __uint_as_float(v & 0xFFFF0000u); }
DI u32 cvtpk(float lo, float hi) {
  typedef __bf16 bf2 __attribute__((ext_vector_type(2)));
  typedef float f2 __attribute__((ext_vector_type(2)));
  f2 f = {lo, hi};
  bf2 r = __builtin_convertvector(f, bf2);
  return __builtin_bit_cast(u32, r);
}
DI u32 nosent(u32 v) { return v < 0xFFFFFFFEu ? v : 0xFFFFFFFEu; }
DI v4u ldnt(const u8* p) { return __builtin_nontemporal_load((const GAS v4u*)p); }
DI f32x4 mfma(v4u a, v4u b, f32x4 c) {
  return __builtin_amdgcn_mfma_f32_16x16x32_bf16(__builtin_bit_cast(bf16x8, a), __builtin_bit_cast(bf16x8, b), c, 0, 0, 0);
}
DI u32 swz16(u32 k) { const u32 k2 = (k >> 2) & 1u, k3 = (k >> 3) & 1u; return k2 | ((k & 3u) << 1) | ((k2 ^ k3) << 3); }
DI void wg_barrier() { asm volatile("s_waitcnt lgkmcnt(0)\n\ts_barrier" ::: "memory"); }
DI void drain_vm() { asm volatile("s_waitcnt vmcnt(0)" ::: "memory"); }
DI float xrow16_max(float x) {
  auto s = __builtin_amdgcn_permlane16_swap(__float_as_uint(x), __float_as_uint(x), false, false);
  x = fmaxf(__uint_as_float(s[0]), __uint_as_float(s[1]));
  auto t = __builtin_amdgcn_permlane32_swap(__float_as_uint(x), __float_as_uint(x), false, false);
  return fmaxf(__uint_as_float(t[0]), __uint_as_float(t[1]));
}
DI float xrow16_sum(float x) {
  auto s = __builtin_amdgcn_permlane16_swap(__float_as_uint(x), __float_as_uint(x), false, false);
  x = __uint_as_float(s[0]) + __uint_as_float(s[1]);
  auto t = __builtin_amdgcn_permlane32_swap(__float_as_uint(x), __float_as_uint(x), false, false);
  return __uint_as_float(t[0]) + __uint_as_float(t[1]);
}
template <int CTRL> DI float dpp(float x) {
  return __builtin_bit_cast(float, __builtin_amdgcn_mov_dpp(__builtin_bit_cast(int, x), CTRL, 0xf, 0xf, true));
}
DI float row16_sum(float v) {
  v += dpp<0xB1>(v); v += dpp<0x4E>(v); v += dpp<0x124>(v); v += dpp<0x128>(v);
  return v;
}
DI float wave_sum(float v) { return xrow16_sum(row16_sum(v)); }
DI void team_bar(LAS u32* cnt, u32 target, int lane) {
  asm volatile("s_waitcnt lgkmcnt(0)" ::: "memory");
  if (lane == 0) __hip_atomic_fetch_add(cnt, 1u, __ATOMIC_RELAXED, __HIP_MEMORY_SCOPE_WORKGROUP);
  while (__hip_atomic_load(cnt, __ATOMIC_RELAXED, __HIP_MEMORY_SCOPE_WORKGROUP) < target) { }
  asm volatile("" ::: "memory");
}
struct Sel { u32 s[2][4]; };
DI Sel make_sel(int l15) {
  Sel r;
  const int jj = l15 & 7;
#pragma unroll
  for (int par = 0; par < 2; ++par) {
    const u32 hb = par ? 0x0302u : 0x0100u;
    const u32 v = (jj & 1) ? ((hb << 16) | 0x0C0Cu) : (0x0C0C0000u | hb);
#pragma unroll
    for (int k = 0; k < 4; ++k) r.s[par][k] = ((jj >> 1) == k) ? v : 0x0C0C0C0Cu;
  }
  return r;
}
DI v4u diagA(const Sel& sel, u32 src, int par) {
  v4u a;
  a.x = __builtin_amdgcn_perm(0u, src, sel.s[par][0]); a.y = __builtin_amdgcn_perm(0u, src, sel.s[par][1]);
  a.z = __builtin_amdgcn_perm(0u, src, sel.s[par][2]); a.w = __builtin_amdgcn_perm(0u, src, sel.s[par][3]);
  return a;
}

#define LD128(off) __builtin_amdgcn_raw_buffer_load_b128(rs, (off), 0, AUX_SC1)
#define LD64(off) __builtin_amdgcn_raw_buffer_load_b64(rs, (off), 0, AUX_SC1)
#define ST128(v, off) __builtin_amdgcn_raw_buffer_store_b128((v), rs, (off), 0, AUX_SC1)
#define ST64(v, off) __builtin_amdgcn_raw_buffer_store_b64((v), rs, (off), 0, AUX_SC1)
#define ST32(v, off) __builtin_amdgcn_raw_buffer_store_b32((v), rs, (off), 0, AUX_SC1)

struct TrB { u32 t0, t1; };
DI TrB tr_base(int lane) {
  const u32 g = lane >> 4, i = lane & 15, q = i >> 2, pp = i & 3;
  const u32 r0 = 8 * g + q, r1 = r0 + 4;
  TrB b;
  b.t0 = r0 * 256 + (((pp >> 1) ^ swz16(r0 & 15)) << 4) + 8 * (pp & 1);
  b.t1 = r1 * 256 + (((pp >> 1) ^ swz16(r1 & 15)) << 4) + 8 * (pp & 1);
  return b;
}
DI bf16x8 tr_frag256(LAS u8* L, const TrB& tb, u32 img_off  , u32 nt) {
  const s16x4 a = __builtin_amdgcn_ds_read_tr16_b64_v4i16((LAS s16x4*)(L + ((tb.t0 ^ (nt << 5)) + img_off)));
  const s16x4 b = __builtin_amdgcn_ds_read_tr16_b64_v4i16((LAS s16x4*)(L + ((tb.t1 ^ (nt << 5)) + img_off)));
  return __builtin_shufflevector(a, b, 0, 1, 2, 3, 4, 5, 6, 7);
}
DI void img256_write(LAS u8* img, int r0, int lane, v4u v) {
  const u32 row = (u32)(r0 + (lane >> 4));
  *(LAS v4u*)(img + row * 256 + (((u32)(lane & 15) ^ swz16(row & 15)) << 4)) = v;
}
DI v4u fr(bf16x8 x) { return __builtin_bit_cast(v4u, x); }
constexpr int WL_XN = 0;
constexpr int WL_Q = 4224;
constexpr int WL_CC = 5376;
constexpr int WL_CX = 6528;
constexpr int WL_IMG = 8192;
static_assert(WL_IMG + 131072 <= LDS_BYTES, "lds");
constexpr int AL_KV = 0;
constexpr int AL_Q = 131072;
constexpr int AL_MX = AL_Q;
constexpr int AL_LS = AL_Q + 1024;
constexpr int AL_P = AL_Q + 2048;
constexpr int AL_XN = 0;
constexpr int AL_RED = 8192;
constexpr int AL_FV = 24576;
constexpr int AL_FH = 26624;
constexpr int AL_FR = 28672;
constexpr int AL_CNT = 31744;
constexpr int AL_OT = 32768;

__global__ __launch_bounds__(NTHR, 2) void mla_ffn_kernel(Params p) {
  extern __shared__ __attribute__((aligned(16))) u8 smem[];
  LAS u8* L = (LAS u8*)smem;
  const int tid = threadIdx.x, lane = tid & 63;
  const int w = __builtin_amdgcn_readfirstlane(tid >> 6);
  const int bid = blockIdx.x, xcd = bid & 7, rr = bid >> 3;
  const __amdgpu_buffer_rsrc_t rs = __builtin_amdgcn_make_buffer_rsrc((void*)p.ws, 0, (int)WS_END, 0x00020000);
  const int g = lane >> 4, l15 = lane & 15;
  STAMP(0);
  const u64 t_start = __builtin_amdgcn_s_memrealtime();
  u32 ep_raw = __builtin_amdgcn_raw_buffer_load_b32(rs, WS_EPOCH, 0, 0);
#define GET_TAG() u32 tag; { u32 e_ = ep_raw; asm volatile("" : "+v"(e_)); tag = e_ + 1u; }
#define GET_TAG_DECL() u32 tag
#define GET_TAG_SET() { u32 e_ = ep_raw; asm volatile("" : "+v"(e_)); tag = e_ + 1u; }

#define WARM_LD(off) __builtin_amdgcn_raw_buffer_load_b32(rs, (off), 0, 0)
#define WARM_USE(x) asm volatile("" :: "v"(x))
  u32 wm0 = 0, wm1 = 0, wm2 = 0;
  if (rr < 16) {
    const int h = xcd * 4 + (rr >> 2), j = rr & 3, wi = h * 4 + j;
    const TrB trb = tr_base(lane);
    const int hf_ = lane >> 5;
    v4u xv = {0, 0, 0, 0};
    if (w < 4) xv = *(const GAS v4u*)(p.x + w * 1024 + lane * 16);
    if (w == 4) wm0 = WARM_LD(WS_QP + (wi & ~3) * 2048 + lane * 128);
    if (w == 5) {
      const v4u sv = {SENT, SENT, SENT, SENT};
      ST128(sv, WS_QA + (g * 32 + h) * 1024 + j * 256 + l15 * 16);
      wm0 = WARM_LD(WS_QF + (lane & 15) * 1024 + (wi >> 5) * 128);
    }
    v4u wqv[4];
#pragma unroll
    for (int i = 0; i < 4; ++i) wqv[i] = ldnt(p.wq + ((size_t)(128 * j + 16 * w + 4 * i + g) * 4096 + h * 128) * 2 + l15 * 16);
    __builtin_amdgcn_sched_barrier(0);
    v4u wuk[4];
    if (w < 4) {
      float v[8] = {bf_lo(xv.x), bf_hi(xv.x), bf_lo(xv.y), bf_hi(xv.y), bf_lo(xv.z), bf_hi(xv.z), bf_lo(xv.w), bf_hi(xv.w)};
      float s = 0.f;
#pragma unroll
      for (int i = 0; i < 8; ++i) s += v[i];
      const float mean = wave_sum(s) * (1.f / 512.f);
      float q2 = 0.f;
#pragma unroll
      for (int i = 0; i < 8; ++i) { v[i] -= mean; q2 += v[i] * v[i]; }
      const float rstd = __builtin_amdgcn_rsqf(wave_sum(q2) * (1.f / 512.f) + 1e-5f);
      v4u o;
      o.x = cvtpk(v[0] * rstd, v[1] * rstd); o.y = cvtpk(v[2] * rstd, v[3] * rstd);
      o.z = cvtpk(v[4] * rstd, v[5] * rstd); o.w = cvtpk(v[6] * rstd, v[7] * rstd);
      *(LAS v4u*)(L + WL_XN + w * 1040 + lane * 16) = o;
    }
#pragma unroll
    for (int i = 0; i < 4; ++i)
      wuk[i] = ldnt(p.wuk + ((size_t)(128 * j + 16 * w + 4 * i + g) * 4096 + h * 128) * 2 + l15 * 16);
    __builtin_amdgcn_sched_barrier(0);
#pragma unroll
    for (int i = 0; i < 4; ++i) img256_write(L + WL_IMG, 16 * w + 4 * i, lane, wqv[i]);
    wg_barrier();
    STAMP(1);
    GET_TAG();
    {
      f32x4 acc = {0, 0, 0, 0};
#pragma unroll
      for (int s = 0; s < 4; ++s) {
        const bf16x8 a = tr_frag256(L, trb, WL_IMG + s * 8192, w);
        const v4u b = *(LAS v4u*)(L + WL_XN + (l15 & 3) * 1040 + (128 * j + 32 * s + 8 * g) * 2);
        acc = mfma(fr(a), b, acc);
      }
      if (l15 < 4) {
        const v2u o = {cvtpk(acc[0], acc[1]), cvtpk(acc[2], acc[3])};
        *(LAS v2u*)(L + WL_CC + l15 * 272 + (16 * w + 4 * g) * 2) = o;
      }
      wg_barrier();
      if (w < 2) {
        const u32 rel = (2 * w + (lane >> 5)) * 512 + (lane & 31) * 16;
        {
          const v2u o = *(LAS v2u*)(L + WL_CC + (2 * w + (lane >> 5)) * 272 + (lane & 31) * 8);
          const v4u o4 = {o.x, tag, o.y, tag};
          ST128(o4, WS_QP + wi * 2048 + rel);
        }
        float t[4];
        for (u32 spin = 0;; ++spin) {
          v4u v[4];
#pragma unroll
          for (int q = 0; q < 4; ++q) v[q] = LD128(WS_QP + (h * 4 + q) * 2048 + rel);
          bool ok = true;
          t[0] = t[1] = t[2] = t[3] = 0.f;
#pragma unroll
          for (int q = 0; q < 4; ++q) {
            ok &= (v[q].y == tag) & (v[q].w == tag);
            t[0] += bf_lo(v[q].x); t[1] += bf_hi(v[q].x); t[2] += bf_lo(v[q].z); t[3] += bf_hi(v[q].z);
          }
          if (__all(ok) || spin > SPIN_CAP) break;
          __builtin_amdgcn_s_sleep(1);
        }
        const v2u o2 = {cvtpk(t[0], t[1]), cvtpk(t[2], t[3])};
        *(LAS v2u*)(L + WL_Q + (2 * w + (lane >> 5)) * 272 + (lane & 31) * 8) = o2;
      }
    }
    v4u ukf[4];
    {
      LAS u8* im = L + WL_IMG + 32768 + w * 4096;
#pragma unroll
      for (int i = 0; i < 4; ++i) img256_write(im, 4 * i, lane, wuk[i]);
#pragma unroll
      for (int s = 0; s < 4; ++s) ukf[s] = *(LAS v4u*)(im + l15 * 256 + (((u32)(4 * s + g) ^ swz16(l15)) << 4));
    }
    WARM_USE(wm0);
    if (w == 5) drain_vm();
    wg_barrier();
    STAMP(2);
    {
      f32x4 acc = {0, 0, 0, 0};
#pragma unroll
      for (int s = 0; s < 4; ++s) acc = mfma(ukf[s], *(LAS v4u*)(L + WL_Q + (l15 & 3) * 272 + (32 * s + 8 * g) * 2), acc);
      constexpr float SC = 0.08838834764831845f * 1.4426950408889634f;
      if (l15 < 4) {
        const v2u o = {cvtpk(acc[0] * SC, acc[1] * SC), cvtpk(acc[2] * SC, acc[3] * SC)};
        *(LAS v2u*)(L + WL_CX + l15 * 272 + (16 * w + 4 * g) * 2) = o;
      }
    }
    wg_barrier();
    if (w == 0) {
      v4u o = *(LAS v4u*)(L + WL_CX + g * 272 + l15 * 16);
      o.x = nosent(o.x); o.y = nosent(o.y); o.z = nosent(o.z); o.w = nosent(o.w);
      ST128(o, WS_QA + (g * 32 + h) * 1024 + j * 256 + l15 * 16);
      if (lane < 16) ST32(tag, WS_QF + lane * 1024 + wi * 4);
    }
    STAMP(3);
    if (w == 6) { wm1 = WARM_LD(lane < 16 ? WS_ST + wi * 2048 + lane * 128 : WS_FP + (wi < 112 ? wi : 0) * 2048 + (lane & 15) * 128); }
    if (w == 7) { wm1 = WARM_LD(WS_OUTP + wi * 8192 + lane * 128); wm2 = WARM_LD(WS_YP + wi * 7168 + (lane < 56 ? lane : 0) * 128); }
    bf16x8 fuv[4], fo[4][4];
    {
      v4u t[4];
#pragma unroll
      for (int i = 0; i < 4; ++i)
        t[i] = ldnt(p.wuv + ((size_t)(128 * j + 16 * w + 4 * i + g) * 4096 + h * 128) * 2 + l15 * 16);
      v4u u[4][4];
#define WO_ISSUE(ss) _Pragma("unroll") for (int i = 0; i < 4; ++i) u[ss][i] = ldnt(p.wo + ((size_t)(h * 128 + 32 * (((ss) + j) & 3) + 4 * w + i) * 512) * 2 + lane * 16); __builtin_amdgcn_sched_barrier(0);
      WO_ISSUE(0)
#pragma unroll
      for (int i = 0; i < 4; ++i) img256_write(L + WL_IMG, 16 * w + 4 * i, lane, t[i]);
      wg_barrier();
#pragma unroll
      for (int s = 0; s < 4; ++s) fuv[s] = tr_frag256(L, trb, WL_IMG + s * 8192, w);
#pragma unroll
      for (int ss = 0; ss < 4; ++ss) {
        if (ss == 0) { WO_ISSUE(1) }
        if (ss == 1) { WO_ISSUE(2) }
        if (ss == 2) { WO_ISSUE(3) }
        LAS u8* img = L + WL_IMG + ((ss + 1) & 3) * 32768;
        if (ss == 3) wg_barrier();
#pragma unroll
        for (int i = 0; i < 4; ++i) {
          const u32 row = 4 * w + i;
          *(LAS v4u*)(img + g * 8192 + row * 256 + (((u32)l15 ^ swz16(row & 15)) << 4)) = u[ss][i];
        }
        wg_barrier();
#pragma unroll
        for (int n = 0; n < 4; ++n) fo[ss][n] = tr_frag256(L, trb, WL_IMG + ((ss + 1) & 3) * 32768 + (w >> 1) * 8192, 4 * (w & 1) + n);
      }
    }
    WARM_USE(wm1); WARM_USE(wm2);
    STAMP(4);
    if (w < 4) {
      const int b = w;
      u32 asc[8];
      float invL;
      {
        float m[8], lsum[8];
        for (u32 spin = 0;; ++spin) {
          bool ok = true;
#pragma unroll
          for (int s = 0; s < 8; ++s) {
            const v4u v = LD128(WS_ST + wi * 2048 + (b * 32 + 4 * s + g) * 16);
            ok &= (v.y == tag) & (v.w == tag);
            m[s] = __uint_as_float(v.x); lsum[s] = __uint_as_float(v.z);
          }
          if (__all(ok) || spin > SPIN_CAP) break;
          __builtin_amdgcn_s_sleep(4);
        }
        float M = m[0];
#pragma unroll
        for (int s = 1; s < 8; ++s) M = fmaxf(M, m[s]);
        M = xrow16_max(M);
        float Ls = 0.f;
#pragma unroll
        for (int s = 0; s < 8; ++s) {
          const u32 e = cvtpk(__builtin_amdgcn_exp2f(m[s] - M), 0.f) & 0xFFFFu;
          asc[s] = e;
          Ls += bf_lo(e) * lsum[s];
        }
        Ls = xrow16_sum(Ls);
        invL = __builtin_amdgcn_rcpf(Ls);
      }
      __builtin_amdgcn_fence(__ATOMIC_ACQUIRE, "wavefront");
      STAMP(5);
      v4u v[8];
      for (u32 spin = 0;; ++spin) {
        bool ok = true;
#pragma unroll
        for (int s = 0; s < 8; ++s) {
          v[s] = LD128(WS_PART + wi * 32768 + ((b * 32 + 4 * s + g) * 128) * 2 + l15 * 16);
          ok &= (v[s].x != SENT) & (v[s].y != SENT) & (v[s].z != SENT) & (v[s].w != SENT);
        }
        if (__all(ok) || spin > SPIN_CAP) break;
        __builtin_amdgcn_s_sleep(1);
      }
      STAMP(6);
      f32x4 acc = {0, 0, 0, 0};
#pragma unroll
      for (int s = 0; s < 8; ++s) {
        v4u a = diagA(make_sel(l15), asc[s], 0);
        if (l15 >= 8) a = v4u{0, 0, 0, 0};
        acc = mfma(a, v[s], acc);
      }
      if (g < 2) {
        const v2u o = {cvtpk(acc[0] * invL, acc[1] * invL), cvtpk(acc[2] * invL, acc[3] * invL)};
        *(LAS v2u*)(L + WL_CC + b * 272 + (32 * (l15 >> 2) + 16 * g + 4 * (l15 & 3)) * 2) = o;
      }
    }
    wg_barrier();
    {
      f32x4 acc = {0, 0, 0, 0};
#pragma unroll
      for (int s = 0; s < 4; ++s) acc = mfma(fr(fuv[s]), *(LAS v4u*)(L + WL_CC + (l15 & 3) * 272 + (32 * s + 8 * g) * 2), acc);
      if (l15 < 4) {
        const v2u o = {cvtpk(acc[0], acc[1]), cvtpk(acc[2], acc[3])};
        *(LAS v2u*)(L + WL_CX + l15 * 272 + (16 * w + 4 * g) * 2) = o;
      }
    }
    wg_barrier();
    {
      v4u bb[4];
#pragma unroll
      for (int ss = 0; ss < 4; ++ss) bb[ss] = *(LAS v4u*)(L + WL_CX + (l15 & 3) * 272 + (32 * ((ss + j) & 3) + 8 * g) * 2);
      f32x4 acc[4];
#pragma unroll
      for (int n = 0; n < 4; ++n) {
        acc[n] = f32x4{0, 0, 0, 0};
#pragma unroll
        for (int ss = 0; ss < 4; ++ss) acc[n] = mfma(fr(fo[ss][n]), bb[ss], acc[n]);
      }
      const int u = l15 >> 2;
      const f32x4 a = u == 0 ? acc[0] : (u == 1 ? acc[1] : (u == 2 ? acc[2] : acc[3]));
      const v4u o = {cvtpk(a[0], a[1]), tag, cvtpk(a[2], a[3]), tag};
      ST128(o, WS_OUTP + (((l15 & 3) * 16 + 2 * w + (u >> 1)) * 128 + wi) * 128 + ((u & 1) * 4 + g) * 16);
    }
    STAMP(7);
    STAMP_FLUSH();
  } else {
    const int a = xcd * 16 + (rr - 16), b = a >> 5, ch = a & 31;
    const int fb = a >> 2, ks4 = a & 3;
    const bool ffn = fb < 28;
    v4u xv = {0, 0, 0, 0};
    const int l31 = lane & 31, hf = lane >> 5;
    GET_TAG_DECL();
    if (w < 4) {
      v4u fa[32];
      {
        constexpr int WIN = CKWIN;
        const u8* src = p.ckv + ((size_t)(b * 4096 + ch * 128 + 32 * w) * 512) * 2 + lane * 16;
        v4u r[WIN];
#pragma unroll
        for (int i = 0; i < WIN; ++i) r[i] = ldnt(src + i * 1024);
        __builtin_amdgcn_sched_barrier(0);
#pragma unroll
        for (int i = 0; i < 32; ++i) {
          const v4u v = r[i % WIN];
          *(LAS v4u*)(L + (((u32)(AL_KV + 32 * w * 1024 + lane * 16) ^ (swz16(i & 15) << 4)) + (u32)(i * 1024))) = v;
          if (i + WIN < 32) r[i % WIN] = ldnt(src + (i + WIN) * 1024);
          __builtin_amdgcn_sched_barrier(0);
        }
        STAMP(1);
        xv = *(const GAS v4u*)(p.x + w * 1024 + lane * 16);
      }
      const u32 fsw = ((u32)hf ^ swz16(l15)) << 4;
      {
        const u32 a0 = (u32)(AL_KV + (32 * w + l31) * 1024) + fsw;
#pragma unroll
        for (int s = 0; s < 32; ++s) fa[s] = *(LAS v4u*)(L + ((a0 ^ (u32)((s & 7) << 5)) + (u32)((s >> 3) * 256)));
      }
      wg_barrier();
      STAMP(4);
      f32x16 S, S2;
#pragma unroll
      for (int i = 0; i < 16; ++i) { S[i] = 0.f; S2[i] = 0.f; }
      {
        v4u bq[2][8];
        const u32 q0 = (u32)(AL_Q + l31 * 1024) + fsw;
#define BQ_LOAD(grp) _Pragma("unroll") for (int i = 0; i < 8; ++i) bq[(grp) & 1][i] = *(LAS v4u*)(L + ((q0 ^ (u32)(i << 5)) + (u32)((grp) * 256)));
        BQ_LOAD(0)
#pragma unroll
        for (int gq = 0; gq < 4; ++gq) {
          if (gq < 3) { BQ_LOAD(gq + 1) }
          __builtin_amdgcn_sched_barrier(0);
#pragma unroll
          for (int i = 0; i < 8; i += 2) {
            S = __builtin_amdgcn_mfma_f32_32x32x16_bf16(__builtin_bit_cast(bf16x8, fa[8 * gq + i]), __builtin_bit_cast(bf16x8, bq[gq & 1][i]), S, 0, 0, 0);
            S2 = __builtin_amdgcn_mfma_f32_32x32x16_bf16(__builtin_bit_cast(bf16x8, fa[8 * gq + i + 1]), __builtin_bit_cast(bf16x8, bq[gq & 1][i + 1]), S2, 0, 0, 0);
          }
          __builtin_amdgcn_sched_barrier(0);
        }
      }
      S += S2;
      STAMP(15);
      v4u f1[8], f2[4];
      if (ffn) {
        const u8* b1 = (l15 < 8 ? p.wg : p.wu) + ((size_t)(128 * ks4 + 32 * w + 8 * g) * 1792 + fb * 64 + (l15 & 7) * 8) * 2;
#pragma unroll
        for (int s = 0; s < 8; ++s) f1[s] = ldnt(b1 + s * 3584);
        __builtin_amdgcn_sched_barrier(0);
      }
      float mt = S[0];
#pragma unroll
      for (int i = 1; i < 16; ++i) mt = fmaxf(mt, S[i]);
      {
        auto t = __builtin_amdgcn_permlane32_swap(__float_as_uint(mt), __float_as_uint(mt), false, false);
        mt = fmaxf(__uint_as_float(t[0]), __uint_as_float(t[1]));
      }
      STAMP(14);
      wg_barrier();
      if (hf == 0) *(LAS float*)(L + AL_MX + (l31 * 4 + w) * 4) = mt;
      if (tid == 0) *(LAS u32*)(L + AL_CNT) = 0;
      wg_barrier();
      STAMP(5);
      const f32x4 m4 = *(LAS f32x4*)(L + AL_MX + l31 * 16);
      const float M = fmaxf(fmaxf(m4[0], m4[1]), fmaxf(m4[2], m4[3]));
      u32 pk[8];
      float ls = 0.f;
#pragma unroll
      for (int i = 0; i < 8; ++i) {
        pk[i] = cvtpk(__builtin_amdgcn_exp2f(S[2 * i] - M), __builtin_amdgcn_exp2f(S[2 * i + 1] - M));
        ls += bf_lo(pk[i]) + bf_hi(pk[i]);
      }
      {
        auto t = __builtin_amdgcn_permlane32_swap(__float_as_uint(ls), __float_as_uint(ls), false, false);
        ls = __uint_as_float(t[0]) + __uint_as_float(t[1]);
      }
#pragma unroll
      for (int u = 0; u < 2; ++u) {
        const v4u o = {pk[4 * u], pk[4 * u + 1], pk[4 * u + 2], pk[4 * u + 3]};
        *(LAS v4u*)(L + AL_P + (((w * 2 + (l31 >> 4)) * 64) + (2 * u + hf) * 16 + l15) * 16) = o;
      }
      if (hf == 0) *(LAS float*)(L + AL_LS + (l31 * 4 + w) * 4) = ls;
      wg_barrier();
      STAMP(6);
      GET_TAG_SET();
      if (w == 3) {
        const f32x4 l4 = *(LAS f32x4*)(L + AL_LS + l31 * 16);
        const float Ls = (l4[0] + l4[1]) + (l4[2] + l4[3]);
        const v4u o = {__float_as_uint(M), tag, __float_as_uint(Ls), tag};
        ST128(o, WS_ST + (l31 * 4 + hf * 2) * 2048 + a * 16);
        ST128(o, WS_ST + (l31 * 4 + hf * 2 + 1) * 2048 + a * 16);
      }
      if (ffn) {
        LAS u32* tb = (LAS u32*)(L + AL_CNT);
        const Sel sel = make_sel(l15);
        {
          const u8* b2 = p.wd + ((size_t)(fb * 64 + 16 * ks4 + 4 * g) * 512 + 128 * w + 8 * l15) * 2;
#pragma unroll
          for (int s = 0; s < 4; ++s) f2[s] = ldnt(b2 + s * 1024);
          __builtin_amdgcn_sched_barrier(0);
        }
        {
          float v[8] = {bf_lo(xv.x), bf_hi(xv.x), bf_lo(xv.y), bf_hi(xv.y), bf_lo(xv.z), bf_hi(xv.z), bf_lo(xv.w), bf_hi(xv.w)};
          float s = 0.f;
#pragma unroll
          for (int i = 0; i < 8; ++i) s += v[i];
          const float mean = wave_sum(s) * (1.f / 512.f);
          float q2 = 0.f;
#pragma unroll
          for (int i = 0; i < 8; ++i) { v[i] -= mean; q2 += v[i] * v[i]; }
          const float rstd = __builtin_amdgcn_rsqf(wave_sum(q2) * (1.f / 512.f) + 1e-5f);
          v4u o;
          o.x = cvtpk(v[0] * rstd, v[1] * rstd); o.y = cvtpk(v[2] * rstd, v[3] * rstd);
          o.z = cvtpk(v[4] * rstd, v[5] * rstd); o.w = cvtpk(v[6] * rstd, v[7] * rstd);
          *(LAS v4u*)(L + AL_XN + w * 1040 + lane * 16) = o;
        }
        team_bar(tb, 4, lane);
#pragma unroll
        for (int pp = 0; pp < 2; ++pp) {
          const v4u xs = *(LAS v4u*)(L + AL_XN + (2 * pp + (l15 >> 3)) * 1040 + (128 * ks4 + 32 * w + 8 * g) * 2);
          f32x4 ac = {0, 0, 0, 0};
#pragma unroll
          for (int s = 0; s < 8; ++s) ac = mfma(diagA(sel, xs[s >> 1], s & 1), f1[s], ac);
          *(LAS f32x4*)(L + AL_RED + ((w * 2 + pp) * 64 + lane) * 16) = ac;
        }
        team_bar(tb, 8, lane);
        if (w < 2) {
          f32x4 s = *(LAS f32x4*)(L + AL_RED + ((0 * 2 + w) * 64 + lane) * 16);
#pragma unroll
          for (int ww = 1; ww < 4; ++ww) s += *(LAS f32x4*)(L + AL_RED + ((ww * 2 + w) * 64 + lane) * 16);
          const u32 rel = (2 * w + (g >> 1)) * 512 + (8 * l15 + 4 * (g & 1)) * 4;
          const v4u o = {cvtpk(s[0], s[1]), tag, cvtpk(s[2], s[3]), tag};
          ST128(o, WS_FP + a * 2048 + rel);
          const u32 off = WS_FP + (a & ~3) * 2048 + rel;
          float t[4];
          for (u32 spin = 0;; ++spin) {
            v4u v[4];
#pragma unroll
            for (int q = 0; q < 4; ++q) v[q] = LD128(off + q * 2048);
            bool ok = true;
            t[0] = t[1] = t[2] = t[3] = 0.f;
#pragma unroll
            for (int q = 0; q < 4; ++q) {
              ok &= (v[q].y == tag) & (v[q].w == tag);
              t[0] += bf_lo(v[q].x); t[1] += bf_hi(v[q].x); t[2] += bf_lo(v[q].z); t[3] += bf_hi(v[q].z);
            }
            if (__all(ok) || spin > SPIN_CAP) break;
            __builtin_amdgcn_s_sleep(2);
          }
          *(LAS f32x4*)(L + AL_FV + ((2 * w + (g >> 1)) * 128 + 8 * l15 + 4 * (g & 1)) * 4) = f32x4{t[0], t[1], t[2], t[3]};
        }
        team_bar(tb, 12, lane);
        STAMP(11);
        if (w == 0) {
          const int bb = g, f = 16 * ks4 + l15;
          const float gv = *(LAS float*)(L + AL_FV + (bb * 128 + f) * 4), uv = *(LAS float*)(L + AL_FV + (bb * 128 + 64 + f) * 4);
          const float hv = gv * __builtin_amdgcn_rcpf(1.f + __expf(-gv)) * uv;
          *(LAS u16*)(L + AL_FH + bb * 64 + l15 * 2) = (u16)cvtpk(hv, 0.f);
        }
        team_bar(tb, 16, lane);
#pragma unroll
        for (int pp = 0; pp < 2; ++pp) {
          const v2u hs = *(LAS v2u*)(L + AL_FH + (2 * pp + (l15 >> 3)) * 64 + (4 * g) * 2);
          f32x4 ac = {0, 0, 0, 0};
#pragma unroll
          for (int s = 0; s < 4; ++s) ac = mfma(diagA(sel, hs[s >> 1], s & 1), f2[s], ac);
          const v4u o = {cvtpk(ac[0], ac[1]), tag, cvtpk(ac[2], ac[3]), tag};
          ST128(o, WS_YP + (((2 * pp + (g >> 1)) * 16 + 4 * w + (l15 >> 2)) * 112 + a) * 128 + (2 * (l15 & 3) + (g & 1)) * 16);
        }
      }
      STAMP(12);
    } else {
      const int w4 = w - 4;
      GET_TAG_SET();
      if (w >= 6) {
        const int h0 = 16 * (w - 6);
        for (u32 spin = 0;; ++spin) {
          const u32 f = __builtin_amdgcn_raw_buffer_load_b32(rs, WS_QF + (a & 15) * 1024 + h0 * 16 + lane * 4, 0, AUX_SC1);
          if (__all(f == tag) || spin > SPIN_CAP) break;
          __builtin_amdgcn_s_sleep(POLLSLEEP);
        }
        __builtin_amdgcn_fence(__ATOMIC_ACQUIRE, "wavefront");
        STAMPW(2, 6);
        for (u32 spin = 0;; ++spin) {
          v4u t[16];
#pragma unroll
          for (int i = 0; i < 16; ++i) t[i] = LD128(WS_QA + (b * 32 + h0 + i) * 1024 + lane * 16);
          bool ok = true;
#pragma unroll
          for (int i = 0; i < 16; ++i) {
            ok &= (t[i].x != SENT) & (t[i].y != SENT) & (t[i].z != SENT) & (t[i].w != SENT);
            *(LAS v4u*)(L + (((u32)(AL_Q + lane * 16) ^ (swz16(i) << 4)) + (u32)((h0 + i) * 1024))) = t[i];
          }
          if (__all(ok) || spin > SPIN_CAP) break;
          __builtin_amdgcn_s_sleep(2);
        }
        STAMPW(3, 6);
      }
      wg_barrier();
      if (w == 5) {
        const v4u sv = {SENT, SENT, SENT, SENT};
#pragma unroll
        for (int i = 0; i < 32; ++i) ST128(sv, WS_PART + (4 * i + g) * 32768 + a * 256 + l15 * 16);
      }
      v4u ft[32];
      {
        const int q = l15 >> 2, pp = l15 & 3, hf2 = g & 1, u2 = g >> 1;
        const u32 r0 = 16 * u2 + 4 * hf2 + q;
        const u32 t0 = (u32)AL_KV + r0 * 1024 + w4 * 256 + ((((u32)pp >> 1) ^ swz16(r0 & 15)) << 4) + 8 * (pp & 1);
        const u32 t1 = (u32)AL_KV + (r0 + 8) * 1024 + w4 * 256 + ((((u32)pp >> 1) ^ swz16((r0 + 8) & 15)) << 4) + 8 * (pp & 1);
#pragma unroll
        for (int ct = 0; ct < 8; ++ct) {
#pragma unroll
          for (int kh = 0; kh < 2; ++kh) {
            const u32 b0 = (t0 ^ (u32)(ct << 5)) + (u32)(kh * 65536), b1 = (t1 ^ (u32)(ct << 5)) + (u32)(kh * 65536);
#pragma unroll
            for (int k2 = 0; k2 < 2; ++k2) {
              const s16x4 x0 = __builtin_amdgcn_ds_read_tr16_b64_v4i16((LAS s16x4*)(L + b0 + k2 * 32768));
              const s16x4 x1 = __builtin_amdgcn_ds_read_tr16_b64_v4i16((LAS s16x4*)(L + b1 + k2 * 32768));
              ft[ct * 4 + 2 * kh + k2] = __builtin_bit_cast(v4u, __builtin_shufflevector(x0, x1, 0, 1, 2, 3, 4, 5, 6, 7));
            }
          }
        }
      }
      STAMPW(13, 4);
      wg_barrier();
      wg_barrier();
      if (w == 5) { drain_vm(); STAMPW(10, 5); }
      wg_barrier();
      v4u pb[2][4];
#pragma unroll
      for (int kt = 0; kt < 4; ++kt)
#pragma unroll
        for (int ht = 0; ht < 2; ++ht) pb[ht][kt] = *(LAS v4u*)(L + AL_P + ((kt * 2 + ht) * 64 + lane) * 16);
#pragma unroll
      for (int T = 0; T < 4; ++T) {
        f32x4 acc[2][2];
#pragma unroll
        for (int c2 = 0; c2 < 2; ++c2)
#pragma unroll
          for (int ht = 0; ht < 2; ++ht) {
            f32x4 z = {0, 0, 0, 0};
#pragma unroll
            for (int kt = 0; kt < 4; ++kt) z = mfma(ft[(2 * T + c2) * 4 + kt], pb[ht][kt], z);
            acc[c2][ht] = z;
          }
#pragma unroll
        for (int ht = 0; ht < 2; ++ht) {
          v4u o;
          o.x = nosent(cvtpk(acc[0][ht][0], acc[0][ht][1])); o.y = nosent(cvtpk(acc[0][ht][2], acc[0][ht][3]));
          o.z = nosent(cvtpk(acc[1][ht][0], acc[1][ht][1])); o.w = nosent(cvtpk(acc[1][ht][2], acc[1][ht][3]));
          *(LAS v4u*)(L + AL_OT + w4 * 8192 + (16 * ht + l15) * 256 + (((u32)(4 * T + g) ^ (u32)l15) << 4)) = o;
        }
      }
#pragma unroll
      for (int i = 0; i < 8; ++i) {
        const u32 hd = 4 * i + g;
        const v4u o = *(LAS v4u*)(L + AL_OT + w4 * 8192 + hd * 256 + (((u32)l15 ^ (hd & 15)) << 4));
        ST128(o, WS_PART + (hd * 4 + w4) * 32768 + a * 256 + l15 * 16);
      }
      STAMPW(7, 4);
    }
    wg_barrier();
    if ((a & 1) == 0) {
      const int fin = a >> 1;
      const int qd = tid & 7;
      const int o_ = fin * 32 + (tid & 31);
      const u32 xraw = *(const GAS u16*)(p.x + o_ * 2);
      v4u vs[2], vy[2];
      vy[1] = v4u{0, tag, 0, tag};
      for (u32 spin = 0;; ++spin) {
        vs[0] = LD128(WS_OUTP + fin * 16384 + tid * 16);
        vs[1] = LD128(WS_OUTP + fin * 16384 + 8192 + tid * 16);
        vy[0] = LD128(WS_YP + fin * 14336 + tid * 16);
        if (tid < 384) vy[1] = LD128(WS_YP + fin * 14336 + 8192 + tid * 16);
        const bool ok = (vs[0].y == tag) & (vs[0].w == tag) & (vs[1].y == tag) & (vs[1].w == tag) & (vy[0].y == tag) & (vy[0].w == tag) & (vy[1].y == tag) & (vy[1].w == tag);
        if (__all(ok) || spin > SPIN_CAP) break;
        __builtin_amdgcn_s_sleep(2);
      }
      STAMP(8);
      float r[8] = {bf_lo(vs[0].x) + bf_lo(vs[1].x), bf_hi(vs[0].x) + bf_hi(vs[1].x), bf_lo(vs[0].z) + bf_lo(vs[1].z), bf_hi(vs[0].z) + bf_hi(vs[1].z),
                    bf_lo(vy[0].x) + bf_lo(vy[1].x), bf_hi(vy[0].x) + bf_hi(vy[1].x), bf_lo(vy[0].z) + bf_lo(vy[1].z), bf_hi(vy[0].z) + bf_hi(vy[1].z)};
#pragma unroll
      for (int i = 0; i < 8; ++i) { float v = r[i]; v += dpp<0x128>(v); r[i] = xrow16_sum(v); }
      if (lane < 8) {
        *(LAS f32x4*)(L + AL_FR + (w * 2 + 0) * 128 + qd * 16) = f32x4{r[0], r[1], r[2], r[3]};
        *(LAS f32x4*)(L + AL_FR + (w * 2 + 1) * 128 + qd * 16) = f32x4{r[4], r[5], r[6], r[7]};
      }
      wg_barrier();
      if (tid < 32) {
        float s = 0.f, y = 0.f;
#pragma unroll
        for (int ww = 0; ww < 8; ++ww) {
          s += *(LAS float*)(L + AL_FR + (ww * 2 + 0) * 128 + tid * 4);
          y += *(LAS float*)(L + AL_FR + (ww * 2 + 1) * 128 + tid * 4);
        }
        const float gt = y * __builtin_amdgcn_rcpf(1.f + __expf(-y));
        *(GAS u16*)(p.out + o_ * 2) = (u16)cvtpk(bf_lo(xraw) + s * gt, 0.f);
      }
      if (a == 0 && tid == 0) ST32(tag, WS_EPOCH);
      STAMP(9);
    }
    STAMP_FLUSH();
  }
}

extern "C" void kernel_launch(void* const* d_in, const int* in_sizes, int n_in, void* d_out, int out_size, void* d_ws, size_t ws_size,
                              hipStream_t stream) {
  Params p;
  p.x = (const u8*)d_in[0]; p.ckv = (const u8*)d_in[1]; p.wq = (const u8*)d_in[2]; p.wuk = (const u8*)d_in[3];
  p.wuv = (const u8*)d_in[4]; p.wo = (const u8*)d_in[5]; p.wg = (const u8*)d_in[7]; p.wu = (const u8*)d_in[8]; p.wd = (const u8*)d_in[9];
  p.out = (u8*)d_out; p.ws = (u8*)d_ws;
  static bool attr = false;
  if (!attr) { (void)hipFuncSetAttribute((const void*)mla_ffn_kernel, hipFuncAttributeMaxDynamicSharedMemorySize, LDS_BYTES); attr = true; }
  hipLaunchKernelGGL(mla_ffn_kernel, dim3(NWG), dim3(NTHR), LDS_BYTES, stream, p);
}
```

```cpp
#include <hip/hip_runtime.h>
#include <stdint.h>

typedef unsigned char u8;
typedef unsigned short u16;
typedef unsigned int u32;
typedef unsigned long long u64;
typedef u32 v4u __attribute__((ext_vector_type(4)));
typedef u32 v2u __attribute__((ext_vector_type(2)));
typedef float f32x4 __attribute__((ext_vector_type(4)));
typedef float f32x16 __attribute__((ext_vector_type(16)));
typedef short bf16x8 __attribute__((ext_vector_type(8)));
typedef short s16x4 __attribute__((ext_vector_type(4)));
#define GAS __attribute__((address_space(1)))
#define LAS __attribute__((address_space(3)))
#define DI __device__ __forceinline__

constexpr int NWG = 256, NTHR = 512;
constexpr u32 SENT = 0xFFFFFFFFu;
constexpr int LDS_BYTES = 163840;

constexpr u32 WS_EPOCH = 0;
constexpr u32 WS_QF = 4096;
constexpr u32 WS_QA = 32768;
constexpr u32 WS_QP = WS_QA + 4 * 32768;
constexpr u32 WS_FP = WS_QP + 128 * 2048;
constexpr u32 WS_ST = WS_FP + 112 * 2048;
constexpr u32 WS_OUTP = WS_ST + 128 * 2048;
constexpr u32 WS_YP = WS_OUTP + 64 * 16384;
constexpr u32 WS_PART = WS_YP + 64 * 14336;
constexpr u32 WS_END = WS_PART + 128 * 32768;
constexpr u32 WS_STAMP = 200u << 20;

constexpr int AUX_SC1 = 16;
constexpr u32 SPIN_CAP = 1u << 17;
#define POLLSLEEP 2
#define ADELAY 20
#define CKWIN 8

#define STAMPW(k, wv) do { } while (0)
#define STAMP_FLUSH() do { } while (0)
#define STAMP(k) STAMPW(k, 0)

struct Params {
  const u8* x; const u8* ckv; const u8* wq; const u8* wuk; const u8* wuv; const u8* wo;
  const u8* wg; const u8* wu; const u8* wd; u8* out; u8* ws;
};

DI float bf_lo(u32 v) { return __uint_as_float(v << 16); }
DI float bf_hi(u32 v) { return __uint_as_float(v & 0xFFFF0000u); }
DI u32 cvtpk(float lo, float hi) {
  typedef __bf16 bf2 __attribute__((ext_vector_type(2)));
  typedef float f2 __attribute__((ext_vector_type(2)));
  f2 f = {lo, hi};
  bf2 r = __builtin_convertvector(f, bf2);
  return __builtin_bit_cast(u32, r);
}
DI u32 nosent(u32 v) { return v < 0xFFFFFFFEu ? v : 0xFFFFFFFEu; }
DI v4u ldnt(const u8* p) { return __builtin_nontemporal_load((const GAS v4u*)p); }
DI f32x4 mfma(v4u a, v4u b, f32x4 c) {
  return __builtin_amdgcn_mfma_f32_16x16x32_bf16(__builtin_bit_cast(bf16x8, a), __builtin_bit_cast(bf16x8, b), c, 0, 0, 0);
}
DI u32 swz16(u32 k) { const u32 k2 = (k >> 2) & 1u, k3 = (k >> 3) & 1u; return k2 | ((k & 3u) << 1) | ((k2 ^ k3) << 3); }
DI void wg_barrier() { asm volatile("s_waitcnt lgkmcnt(0)\n\ts_barrier" ::: "memory"); }
DI void drain_vm() { asm volatile("s_waitcnt vmcnt(0)" ::: "memory"); }
DI float xrow16_max(float x) {
  auto s = __builtin_amdgcn_permlane16_swap(__float_as_uint(x), __float_as_uint(x), false, false);
  x = fmaxf(__uint_as_float(s[0]), __uint_as_float(s[1]));
  auto t = __builtin_amdgcn_permlane32_swap(__float_as_uint(x), __float_as_uint(x), false, false);
  return fmaxf(__uint_as_float(t[0]), __uint_as_float(t[1]));
}
DI float xrow16_sum(float x) {
  auto s = __builtin_amdgcn_permlane16_swap(__float_as_uint(x), __float_as_uint(x), false, false);
  x = __uint_as_float(s[0]) + __uint_as_float(s[1]);
  auto t = __builtin_amdgcn_permlane32_swap(__float_as_uint(x), __float_as_uint(x), false, false);
  return __uint_as_float(t[0]) + __uint_as_float(t[1]);
}
template <int CTRL> DI float dpp(float x) {
  return __builtin_bit_cast(float, __builtin_amdgcn_mov_dpp(__builtin_bit_cast(int, x), CTRL, 0xf, 0xf, true));
}
DI float row16_sum(float v) {
  v += dpp<0xB1>(v); v += dpp<0x4E>(v); v += dpp<0x124>(v); v += dpp<0x128>(v);
  return v;
}
DI float wave_sum(float v) { return xrow16_sum(row16_sum(v)); }
DI void team_bar(LAS u32* cnt, u32 target, int lane) {
  asm volatile("s_waitcnt lgkmcnt(0)" ::: "memory");
  if (lane == 0) __hip_atomic_fetch_add(cnt, 1u, __ATOMIC_RELAXED, __HIP_MEMORY_SCOPE_WORKGROUP);
  while (__hip_atomic_load(cnt, __ATOMIC_RELAXED, __HIP_MEMORY_SCOPE_WORKGROUP) < target) { }
  asm volatile("" ::: "memory");
}
struct Sel { u32 s[2][4]; };
DI Sel make_sel(int l15) {
  Sel r;
  const int jj = l15 & 7;
#pragma unroll
  for (int par = 0; par < 2; ++par) {
    const u32 hb = par ? 0x0302u : 0x0100u;
    const u32 v = (jj & 1) ? ((hb << 16) | 0x0C0Cu) : (0x0C0C0000u | hb);
#pragma unroll
    for (int k = 0; k < 4; ++k) r.s[par][k] = ((jj >> 1) == k) ? v : 0x0C0C0C0Cu;
  }
  return r;
}
DI v4u diagA(const Sel& sel, u32 src, int par) {
  v4u a;
  a.x = __builtin_amdgcn_perm(0u, src, sel.s[par][0]); a.y = __builtin_amdgcn_perm(0u, src, sel.s[par][1]);
  a.z = __builtin_amdgcn_perm(0u, src, sel.s[par][2]); a.w = __builtin_amdgcn_perm(0u, src, sel.s[par][3]);
  return a;
}

#define LD128(off) __builtin_amdgcn_raw_buffer_load_b128(rs, (off), 0, AUX_SC1)
#define LD64(off) __builtin_amdgcn_raw_buffer_load_b64(rs, (off), 0, AUX_SC1)
#define ST128(v, off) __builtin_amdgcn_raw_buffer_store_b128((v), rs, (off), 0, AUX_SC1)
#define ST64(v, off) __builtin_amdgcn_raw_buffer_store_b64((v), rs, (off), 0, AUX_SC1)
#define ST32(v, off) __builtin_amdgcn_raw_buffer_store_b32((v), rs, (off), 0, AUX_SC1)

struct TrB { u32 t0, t1; };
DI TrB tr_base(int lane) {
  const u32 g = lane >> 4, i = lane & 15, q = i >> 2, pp = i & 3;
  const u32 r0 = 8 * g + q, r1 = r0 + 4;
  TrB b;
  b.t0 = r0 * 256 + (((pp >> 1) ^ swz16(r0 & 15)) << 4) + 8 * (pp & 1);
  b.t1 = r1 * 256 + (((pp >> 1) ^ swz16(r1 & 15)) << 4) + 8 * (pp & 1);
  return b;
}
DI bf16x8 tr_frag256(LAS u8* L, const TrB& tb, u32 img_off  , u32 nt) {
  const s16x4 a = __builtin_amdgcn_ds_read_tr16_b64_v4i16((LAS s16x4*)(L + ((tb.t0 ^ (nt << 5)) + img_off)));
  const s16x4 b = __builtin_amdgcn_ds_read_tr16_b64_v4i16((LAS s16x4*)(L + ((tb.t1 ^ (nt << 5)) + img_off)));
  return __builtin_shufflevector(a, b, 0, 1, 2, 3, 4, 5, 6, 7);
}
DI void img256_write(LAS u8* img, int r0, int lane, v4u v) {
  const u32 row = (u32)(r0 + (lane >> 4));
  *(LAS v4u*)(img + row * 256 + (((u32)(lane & 15) ^ swz16(row & 15)) << 4)) = v;
}
DI v4u fr(bf16x8 x) { return __builtin_bit_cast(v4u, x); }
constexpr int WL_XN = 0;
constexpr int WL_Q = 4224;
constexpr int WL_CC = 5376;
constexpr int WL_CX = 6528;
constexpr int WL_IMG = 8192;
static_assert(WL_IMG + 131072 <= LDS_BYTES, "lds");
constexpr int AL_KV = 0;
constexpr int AL_Q = 131072;
constexpr int AL_MX = AL_Q;
constexpr int AL_LS = AL_Q + 1024;
constexpr int AL_P = AL_Q + 2048;
constexpr int AL_XN = 0;
constexpr int AL_RED = 8192;
constexpr int AL_FV = 24576;
constexpr int AL_FH = 26624;
constexpr int AL_FR = 28672;
constexpr int AL_CNT = 31744;
constexpr int AF_XN = AL_Q, AF_RED = AL_Q + 4352, AF_FV = AF_RED + 8192, AF_FH = AF_FV + 2048, AF_CNT = AF_FH + 512;
constexpr int AL_OT = 32768;

__global__ __launch_bounds__(NTHR, 2) void mla_ffn_kernel(Params p) {
  extern __shared__ __attribute__((aligned(16))) u8 smem[];
  LAS u8* L = (LAS u8*)smem;
  const int tid = threadIdx.x, lane = tid & 63;
  const int w = __builtin_amdgcn_readfirstlane(tid >> 6);
  const int bid = blockIdx.x, xcd = bid & 7, rr = bid >> 3;
  const __amdgpu_buffer_rsrc_t rs = __builtin_amdgcn_make_buffer_rsrc((void*)p.ws, 0, (int)WS_END, 0x00020000);
  const int g = lane >> 4, l15 = lane & 15;
  STAMP(0);
  u32 ep_raw = __builtin_amdgcn_raw_buffer_load_b32(rs, WS_EPOCH, 0, 0);
#define GET_TAG() u32 tag; { u32 e_ = ep_raw; asm volatile("" : "+v"(e_)); tag = e_ + 1u; }
#define GET_TAG_DECL() u32 tag
#define GET_TAG_SET() { u32 e_ = ep_raw; asm volatile("" : "+v"(e_)); tag = e_ + 1u; }

#define WARM_LD(off) __builtin_amdgcn_raw_buffer_load_b32(rs, (off), 0, 0)
#define WARM_USE(x) asm volatile("" :: "v"(x))
  u32 wm0 = 0, wm1 = 0, wm2 = 0;
  if (rr < 16) {
    const int h = xcd * 4 + (rr >> 2), j = rr & 3, wi = h * 4 + j;
    const TrB trb = tr_base(lane);
    const int hf_ = lane >> 5;
    v4u xv = {0, 0, 0, 0};
    if (w < 4) xv = *(const GAS v4u*)(p.x + w * 1024 + lane * 16);
    if (w == 4) wm0 = WARM_LD(WS_QP + (wi & ~3) * 2048 + lane * 128);
    if (w == 5) {
      const v4u sv = {SENT, SENT, SENT, SENT};
      ST128(sv, WS_QA + (g * 32 + h) * 1024 + j * 256 + l15 * 16);
      wm0 = WARM_LD(WS_QF + (lane & 15) * 1024 + (wi >> 5) * 128);
    }
    v4u wqv[4];
#pragma unroll
    for (int i = 0; i < 4; ++i) wqv[i] = ldnt(p.wq + ((size_t)(128 * j + 16 * w + 4 * i + g) * 4096 + h * 128) * 2 + l15 * 16);
    __builtin_amdgcn_sched_barrier(0);
    v4u wuk[4];
    if (w < 4) {
      float v[8] = {bf_lo(xv.x), bf_hi(xv.x), bf_lo(xv.y), bf_hi(xv.y), bf_lo(xv.z), bf_hi(xv.z), bf_lo(xv.w), bf_hi(xv.w)};
      float s = 0.f;
#pragma unroll
      for (int i = 0; i < 8; ++i) s += v[i];
      const float mean = wave_sum(s) * (1.f / 512.f);
      float q2 = 0.f;
#pragma unroll
      for (int i = 0; i < 8; ++i) { v[i] -= mean; q2 += v[i] * v[i]; }
      const float rstd = __builtin_amdgcn_rsqf(wave_sum(q2) * (1.f / 512.f) + 1e-5f);
      v4u o;
      o.x = cvtpk(v[0] * rstd, v[1] * rstd); o.y = cvtpk(v[2] * rstd, v[3] * rstd);
      o.z = cvtpk(v[4] * rstd, v[5] * rstd); o.w = cvtpk(v[6] * rstd, v[7] * rstd);
      *(LAS v4u*)(L + WL_XN + w * 1040 + lane * 16) = o;
    }
#pragma unroll
    for (int i = 0; i < 4; ++i)
      wuk[i] = ldnt(p.wuk + ((size_t)(128 * j + 16 * w + 4 * i + g) * 4096 + h * 128) * 2 + l15 * 16);
    __builtin_amdgcn_sched_barrier(0);
#pragma unroll
    for (int i = 0; i < 4; ++i) img256_write(L + WL_IMG, 16 * w + 4 * i, lane, wqv[i]);
    wg_barrier();
    STAMP(1);
    GET_TAG();
    {
      f32x4 acc = {0, 0, 0, 0};
#pragma unroll
      for (int s = 0; s < 4; ++s) {
        const bf16x8 a = tr_frag256(L, trb, WL_IMG + s * 8192, w);
        const v4u b = *(LAS v4u*)(L + WL_XN + (l15 & 3) * 1040 + (128 * j + 32 * s + 8 * g) * 2);
        acc = mfma(fr(a), b, acc);
      }
      if (l15 < 4) {
        const v2u o = {cvtpk(acc[0], acc[1]), cvtpk(acc[2], acc[3])};
        *(LAS v2u*)(L + WL_CC + l15 * 272 + (16 * w + 4 * g) * 2) = o;
      }
      wg_barrier();
      if (w < 2) {
        const u32 rel = (2 * w + (lane >> 5)) * 512 + (lane & 31) * 16;
        {
          const v2u o = *(LAS v2u*)(L + WL_CC + (2 * w + (lane >> 5)) * 272 + (lane & 31) * 8);
          const v4u o4 = {o.x, tag, o.y, tag};
          ST128(o4, WS_QP + wi * 2048 + rel);
        }
        float t[4];
        for (u32 spin = 0;; ++spin) {
          v4u v[4];
#pragma unroll
          for (int q = 0; q < 4; ++q) v[q] = LD128(WS_QP + (h * 4 + q) * 2048 + rel);
          bool ok = true;
          t[0] = t[1] = t[2] = t[3] = 0.f;
#pragma unroll
          for (int q = 0; q < 4; ++q) {
            ok &= (v[q].y == tag) & (v[q].w == tag);
            t[0] += bf_lo(v[q].x); t[1] += bf_hi(v[q].x); t[2] += bf_lo(v[q].z); t[3] += bf_hi(v[q].z);
          }
          if (__all(ok) || spin > SPIN_CAP) break;
          __builtin_amdgcn_s_sleep(1);
        }
        const v2u o2 = {cvtpk(t[0], t[1]), cvtpk(t[2], t[3])};
        *(LAS v2u*)(L + WL_Q + (2 * w + (lane >> 5)) * 272 + (lane & 31) * 8) = o2;
      }
    }
    v4u ukf[4];
    {
      LAS u8* im = L + WL_IMG + 32768 + w * 4096;
#pragma unroll
      for (int i = 0; i < 4; ++i) img256_write(im, 4 * i, lane, wuk[i]);
#pragma unroll
      for (int s = 0; s < 4; ++s) ukf[s] = *(LAS v4u*)(im + l15 * 256 + (((u32)(4 * s + g) ^ swz16(l15)) << 4));
    }
    WARM_USE(wm0);
    if (w == 5) drain_vm();
    wg_barrier();
    STAMP(2);
    {
      f32x4 acc = {0, 0, 0, 0};
#pragma unroll
      for (int s = 0; s < 4; ++s) acc = mfma(ukf[s], *(LAS v4u*)(L + WL_Q + (l15 & 3) * 272 + (32 * s + 8 * g) * 2), acc);
      constexpr float SC = 0.08838834764831845f * 1.4426950408889634f;
      if (l15 < 4) {
        const v2u o = {cvtpk(acc[0] * SC, acc[1] * SC), cvtpk(acc[2] * SC, acc[3] * SC)};
        *(LAS v2u*)(L + WL_CX + l15 * 272 + (16 * w + 4 * g) * 2) = o;
      }
    }
    wg_barrier();
    if (w == 0) {
      v4u o = *(LAS v4u*)(L + WL_CX + g * 272 + l15 * 16);
      o.x = nosent(o.x); o.y = nosent(o.y); o.z = nosent(o.z); o.w = nosent(o.w);
      ST128(o, WS_QA + (g * 32 + h) * 1024 + j * 256 + l15 * 16);
      if (lane < 16) ST32(tag, WS_QF + lane * 1024 + wi * 4);
    }
    STAMP(3);
    if (w == 6) { wm1 = WARM_LD(lane < 16 ? WS_ST + wi * 2048 + lane * 128 : WS_FP + (wi < 112 ? wi : 0) * 2048 + (lane & 15) * 128); }
    if (w == 7) { wm1 = WARM_LD(WS_OUTP + wi * 8192 + lane * 128); wm2 = WARM_LD(WS_YP + wi * 7168 + (lane < 56 ? lane : 0) * 128); }
    bf16x8 fuv[4], fo[4][4];
    {
      v4u t[4];
#pragma unroll
      for (int i = 0; i < 4; ++i)
        t[i] = ldnt(p.wuv + ((size_t)(128 * j + 16 * w + 4 * i + g) * 4096 + h * 128) * 2 + l15 * 16);
      v4u u[4][4];
#define WO_ISSUE(ss) _Pragma("unroll") for (int i = 0; i < 4; ++i) u[ss][i] = ldnt(p.wo + ((size_t)(h * 128 + 32 * (((ss) + j) & 3) + 4 * w + i) * 512) * 2 + lane * 16); __builtin_amdgcn_sched_barrier(0);
      WO_ISSUE(0)
#pragma unroll
      for (int i = 0; i < 4; ++i) img256_write(L + WL_IMG, 16 * w + 4 * i, lane, t[i]);
      wg_barrier();
#pragma unroll
      for (int s = 0; s < 4; ++s) fuv[s] = tr_frag256(L, trb, WL_IMG + s * 8192, w);
#pragma unroll
      for (int ss = 0; ss < 4; ++ss) {
        if (ss == 0) { WO_ISSUE(1) }
        if (ss == 1) { WO_ISSUE(2) }
        if (ss == 2) { WO_ISSUE(3) }
        LAS u8* img = L + WL_IMG + ((ss + 1) & 3) * 32768;
        if (ss == 3) wg_barrier();
#pragma unroll
        for (int i = 0; i < 4; ++i) {
          const u32 row = 4 * w + i;
          *(LAS v4u*)(img + g * 8192 + row * 256 + (((u32)l15 ^ swz16(row & 15)) << 4)) = u[ss][i];
        }
        wg_barrier();
#pragma unroll
        for (int n = 0; n < 4; ++n) fo[ss][n] = tr_frag256(L, trb, WL_IMG + ((ss + 1) & 3) * 32768 + (w >> 1) * 8192, 4 * (w & 1) + n);
      }
    }
    WARM_USE(wm1); WARM_USE(wm2);
    STAMP(4);
    if (w < 4) {
      const int b = w;
      u32 asc[8];
      float invL;
      {
        float m[8], lsum[8];
        for (u32 spin = 0;; ++spin) {
          bool ok = true;
#pragma unroll
          for (int s = 0; s < 8; ++s) {
            const v4u v = LD128(WS_ST + wi * 2048 + (b * 32 + 4 * s + g) * 16);
            ok &= (v.y == tag) & (v.w == tag);
            m[s] = __uint_as_float(v.x); lsum[s] = __uint_as_float(v.z);
          }
          if (__all(ok) || spin > SPIN_CAP) break;
          __builtin_amdgcn_s_sleep(4);
        }
        float M = m[0];
#pragma unroll
        for (int s = 1; s < 8; ++s) M = fmaxf(M, m[s]);
        M = xrow16_max(M);
        float Ls = 0.f;
#pragma unroll
        for (int s = 0; s < 8; ++s) {
          const u32 e = cvtpk(__builtin_amdgcn_exp2f(m[s] - M), 0.f) & 0xFFFFu;
          asc[s] = e;
          Ls += bf_lo(e) * lsum[s];
        }
        Ls = xrow16_sum(Ls);
        invL = __builtin_amdgcn_rcpf(Ls);
      }
      __builtin_amdgcn_fence(__ATOMIC_ACQUIRE, "wavefront");
      STAMP(5);
      v4u v[8];
      for (u32 spin = 0;; ++spin) {
        bool ok = true;
#pragma unroll
        for (int s = 0; s < 8; ++s) {
          v[s] = LD128(WS_PART + wi * 32768 + ((b * 32 + 4 * s + g) * 128) * 2 + l15 * 16);
          ok &= (v[s].x != SENT) & (v[s].y != SENT) & (v[s].z != SENT) & (v[s].w != SENT);
        }
        if (__all(ok) || spin > SPIN_CAP) break;
        __builtin_amdgcn_s_sleep(1);
      }
      STAMP(6);
      f32x4 acc = {0, 0, 0, 0};
#pragma unroll
      for (int s = 0; s < 8; ++s) {
        v4u a = diagA(make_sel(l15), asc[s], 0);
        if (l15 >= 8) a = v4u{0, 0, 0, 0};
        acc = mfma(a, v[s], acc);
      }
      if (g < 2) {
        const v2u o = {cvtpk(acc[0] * invL, acc[1] * invL), cvtpk(acc[2] * invL, acc[3] * invL)};
        *(LAS v2u*)(L + WL_CC + b * 272 + (32 * (l15 >> 2) + 16 * g + 4 * (l15 & 3)) * 2) = o;
      }
    }
    wg_barrier();
    {
      f32x4 acc = {0, 0, 0, 0};
#pragma unroll
      for (int s = 0; s < 4; ++s) acc = mfma(fr(fuv[s]), *(LAS v4u*)(L + WL_CC + (l15 & 3) * 272 + (32 * s + 8 * g) * 2), acc);
      if (l15 < 4) {
        const v2u o = {cvtpk(acc[0], acc[1]), cvtpk(acc[2], acc[3])};
        *(LAS v2u*)(L + WL_CX + l15 * 272 + (16 * w + 4 * g) * 2) = o;
      }
    }
    wg_barrier();
    {
      v4u bb[4];
#pragma unroll
      for (int ss = 0; ss < 4; ++ss) bb[ss] = *(LAS v4u*)(L + WL_CX + (l15 & 3) * 272 + (32 * ((ss + j) & 3) + 8 * g) * 2);
      f32x4 acc[4];
#pragma unroll
      for (int n = 0; n < 4; ++n) {
        acc[n] = f32x4{0, 0, 0, 0};
#pragma unroll
        for (int ss = 0; ss < 4; ++ss) acc[n] = mfma(fr(fo[ss][n]), bb[ss], acc[n]);
      }
      const int u = l15 >> 2;
      const f32x4 a = u == 0 ? acc[0] : (u == 1 ? acc[1] : (u == 2 ? acc[2] : acc[3]));
      const v4u o = {cvtpk(a[0], a[1]), tag, cvtpk(a[2], a[3]), tag};
      ST128(o, WS_OUTP + (((l15 & 3) * 16 + 2 * w + (u >> 1)) * 128 + wi) * 128 + ((u & 1) * 4 + g) * 16);
    }
    STAMP(7);
    STAMP_FLUSH();
  } else {
    const int a = xcd * 16 + (rr - 16), b = a >> 5, ch = a & 31;
    const int fb = a >> 2, ks4 = a & 3;
    const bool ffn = fb < 28;
    v4u xv = {0, 0, 0, 0};
    const int l31 = lane & 31, hf = lane >> 5;
    GET_TAG_DECL();
    if (w < 4) {
      v4u fa[32];
      {
        constexpr int WIN = CKWIN;
        const u8* src = p.ckv + ((size_t)(b * 4096 + ch * 128 + 32 * w) * 512) * 2 + lane * 16;
        __builtin_amdgcn_s_sleep(ADELAY);
        v4u r[WIN];
#pragma unroll
        for (int i = 0; i < WIN; ++i) r[i] = ldnt(src + i * 1024);
        __builtin_amdgcn_sched_barrier(0);
        if (tid == 0) *(LAS u32*)(L + AF_CNT) = 0;
        wg_barrier();
#pragma unroll
        for (int i = 0; i < 32; ++i) {
          const v4u v = r[i % WIN];
          *(LAS v4u*)(L + (((u32)(AL_KV + 32 * w * 1024 + lane * 16) ^ (swz16(i & 15) << 4)) + (u32)(i * 1024))) = v;
          if (i + WIN < 32) r[i % WIN] = ldnt(src + (i + WIN) * 1024);
          __builtin_amdgcn_sched_barrier(0);
        }
        STAMP(1);
      }
      const u32 fsw = ((u32)hf ^ swz16(l15)) << 4;
      {
        const u32 a0 = (u32)(AL_KV + (32 * w + l31) * 1024) + fsw;
#pragma unroll
        for (int s = 0; s < 32; ++s) fa[s] = *(LAS v4u*)(L + ((a0 ^ (u32)((s & 7) << 5)) + (u32)((s >> 3) * 256)));
      }
      wg_barrier();
      STAMP(4);
      f32x16 S, S2;
#pragma unroll
      for (int i = 0; i < 16; ++i) { S[i] = 0.f; S2[i] = 0.f; }
      {
        v4u bq[2][8];
        const u32 q0 = (u32)(AL_Q + l31 * 1024) + fsw;
#define BQ_LOAD(grp) _Pragma("unroll") for (int i = 0; i < 8; ++i) bq[(grp) & 1][i] = *(LAS v4u*)(L + ((q0 ^ (u32)(i << 5)) + (u32)((grp) * 256)));
        BQ_LOAD(0)
#pragma unroll
        for (int gq = 0; gq < 4; ++gq) {
          if (gq < 3) { BQ_LOAD(gq + 1) }
          __builtin_amdgcn_sched_barrier(0);
#pragma unroll
          for (int i = 0; i < 8; i += 2) {
            S = __builtin_amdgcn_mfma_f32_32x32x16_bf16(__builtin_bit_cast(bf16x8, fa[8 * gq + i]), __builtin_bit_cast(bf16x8, bq[gq & 1][i]), S, 0, 0, 0);
            S2 = __builtin_amdgcn_mfma_f32_32x32x16_bf16(__builtin_bit_cast(bf16x8, fa[8 * gq + i + 1]), __builtin_bit_cast(bf16x8, bq[gq & 1][i + 1]), S2, 0, 0, 0);
          }
          __builtin_amdgcn_sched_barrier(0);
        }
      }
      S += S2;
      STAMP(15);
      float mt = S[0];
#pragma unroll
      for (int i = 1; i < 16; ++i) mt = fmaxf(mt, S[i]);
      {
        auto t = __builtin_amdgcn_permlane32_swap(__float_as_uint(mt), __float_as_uint(mt), false, false);
        mt = fmaxf(__uint_as_float(t[0]), __uint_as_float(t[1]));
      }
      STAMP(14);
      wg_barrier();
      if (hf == 0) *(LAS float*)(L + AL_MX + (l31 * 4 + w) * 4) = mt;
      if (tid == 0) *(LAS u32*)(L + AL_CNT) = 0;
      wg_barrier();
      STAMP(5);
      const f32x4 m4 = *(LAS f32x4*)(L + AL_MX + l31 * 16);
      const float M = fmaxf(fmaxf(m4[0], m4[1]), fmaxf(m4[2], m4[3]));
      u32 pk[8];
      float ls = 0.f;
#pragma unroll
      for (int i = 0; i < 8; ++i) {
        pk[i] = cvtpk(__builtin_amdgcn_exp2f(S[2 * i] - M), __builtin_amdgcn_exp2f(S[2 * i + 1] - M));
        ls += bf_lo(pk[i]) + bf_hi(pk[i]);
      }
      {
        auto t = __builtin_amdgcn_permlane32_swap(__float_as_uint(ls), __float_as_uint(ls), false, false);
        ls = __uint_as_float(t[0]) + __uint_as_float(t[1]);
      }
#pragma unroll
      for (int u = 0; u < 2; ++u) {
        const v4u o = {pk[4 * u], pk[4 * u + 1], pk[4 * u + 2], pk[4 * u + 3]};
        *(LAS v4u*)(L + AL_P + (((w * 2 + (l31 >> 4)) * 64) + (2 * u + hf) * 16 + l15) * 16) = o;
      }
      if (hf == 0) *(LAS float*)(L + AL_LS + (l31 * 4 + w) * 4) = ls;
      wg_barrier();
      STAMP(6);
      GET_TAG_SET();
      if (w == 3) {
        const f32x4 l4 = *(LAS f32x4*)(L + AL_LS + l31 * 16);
        const float Ls = (l4[0] + l4[1]) + (l4[2] + l4[3]);
        const v4u o = {__float_as_uint(M), tag, __float_as_uint(Ls), tag};
        ST128(o, WS_ST + (l31 * 4 + hf * 2) * 2048 + a * 16);
        ST128(o, WS_ST + (l31 * 4 + hf * 2 + 1) * 2048 + a * 16);
      }
    } else {
      const int w4 = w - 4;
      if (ffn) {
        LAS u32* tb = (LAS u32*)(L + AF_CNT);
        const Sel sel = make_sel(l15);
        const v4u xv4 = *(const GAS v4u*)(p.x + w4 * 1024 + lane * 16);
        v4u f1[8], f2[4];
        {
          const u8* b1 = (l15 < 8 ? p.wg : p.wu) + ((size_t)(128 * ks4 + 32 * w4 + 8 * g) * 1792 + fb * 64 + (l15 & 7) * 8) * 2;
#pragma unroll
          for (int s = 0; s < 8; ++s) f1[s] = ldnt(b1 + s * 3584);
          __builtin_amdgcn_sched_barrier(0);
        }
        {
          float v[8] = {bf_lo(xv4.x), bf_hi(xv4.x), bf_lo(xv4.y), bf_hi(xv4.y), bf_lo(xv4.z), bf_hi(xv4.z), bf_lo(xv4.w), bf_hi(xv4.w)};
          float s = 0.f;
#pragma unroll
          for (int i = 0; i < 8; ++i) s += v[i];
          const float mean = wave_sum(s) * (1.f / 512.f);
          float q2 = 0.f;
#pragma unroll
          for (int i = 0; i < 8; ++i) { v[i] -= mean; q2 += v[i] * v[i]; }
          const float rstd = __builtin_amdgcn_rsqf(wave_sum(q2) * (1.f / 512.f) + 1e-5f);
          v4u o;
          o.x = cvtpk(v[0] * rstd, v[1] * rstd); o.y = cvtpk(v[2] * rstd, v[3] * rstd);
          o.z = cvtpk(v[4] * rstd, v[5] * rstd); o.w = cvtpk(v[6] * rstd, v[7] * rstd);
          *(LAS v4u*)(L + AF_XN + w4 * 1040 + lane * 16) = o;
        }
        {
          const u8* b2 = p.wd + ((size_t)(fb * 64 + 16 * ks4 + 4 * g) * 512 + 128 * w4 + 8 * l15) * 2;
#pragma unroll
          for (int s = 0; s < 4; ++s) f2[s] = ldnt(b2 + s * 1024);
          __builtin_amdgcn_sched_barrier(0);
        }
        wg_barrier();
        GET_TAG_SET();
#pragma unroll
        for (int pp = 0; pp < 2; ++pp) {
          const v4u xs = *(LAS v4u*)(L + AF_XN + (2 * pp + (l15 >> 3)) * 1040 + (128 * ks4 + 32 * w4 + 8 * g) * 2);
          f32x4 ac = {0, 0, 0, 0};
#pragma unroll
          for (int s = 0; s < 8; ++s) ac = mfma(diagA(sel, xs[s >> 1], s & 1), f1[s], ac);
          *(LAS f32x4*)(L + AF_RED + ((w4 * 2 + pp) * 64 + lane) * 16) = ac;
        }
        team_bar(tb, 4, lane);
        if (w4 < 2) {
          f32x4 s = *(LAS f32x4*)(L + AF_RED + ((0 * 2 + w4) * 64 + lane) * 16);
#pragma unroll
          for (int ww = 1; ww < 4; ++ww) s += *(LAS f32x4*)(L + AF_RED + ((ww * 2 + w4) * 64 + lane) * 16);
          const u32 rel = (2 * w4 + (g >> 1)) * 512 + (8 * l15 + 4 * (g & 1)) * 4;
          const v4u o = {cvtpk(s[0], s[1]), tag, cvtpk(s[2], s[3]), tag};
          ST128(o, WS_FP + a * 2048 + rel);
          const u32 off = WS_FP + (a & ~3) * 2048 + rel;
          float t[4];
          for (u32 spin = 0;; ++spin) {
            v4u v[4];
#pragma unroll
            for (int q = 0; q < 4; ++q) v[q] = LD128(off + q * 2048);
            bool ok = true;
            t[0] = t[1] = t[2] = t[3] = 0.f;
#pragma unroll
            for (int q = 0; q < 4; ++q) {
              ok &= (v[q].y == tag) & (v[q].w == tag);
              t[0] += bf_lo(v[q].x); t[1] += bf_hi(v[q].x); t[2] += bf_lo(v[q].z); t[3] += bf_hi(v[q].z);
            }
            if (__all(ok) || spin > SPIN_CAP) break;
            __builtin_amdgcn_s_sleep(4);
          }
          *(LAS f32x4*)(L + AF_FV + ((2 * w4 + (g >> 1)) * 128 + 8 * l15 + 4 * (g & 1)) * 4) = f32x4{t[0], t[1], t[2], t[3]};
        }
        team_bar(tb, 8, lane);
        STAMPW(11, 4);
        if (w4 == 0) {
          const int bb = g, f = 16 * ks4 + l15;
          const float gv = *(LAS float*)(L + AF_FV + (bb * 128 + f) * 4), uv = *(LAS float*)(L + AF_FV + (bb * 128 + 64 + f) * 4);
          const float hv = gv * __builtin_amdgcn_rcpf(1.f + __expf(-gv)) * uv;
          *(LAS u16*)(L + AF_FH + bb * 64 + l15 * 2) = (u16)cvtpk(hv, 0.f);
        }
        team_bar(tb, 12, lane);
#pragma unroll
        for (int pp = 0; pp < 2; ++pp) {
          const v2u hs = *(LAS v2u*)(L + AF_FH + (2 * pp + (l15 >> 3)) * 64 + (4 * g) * 2);
          f32x4 ac = {0, 0, 0, 0};
#pragma unroll
          for (int s = 0; s < 4; ++s) ac = mfma(diagA(sel, hs[s >> 1], s & 1), f2[s], ac);
          const v4u o = {cvtpk(ac[0], ac[1]), tag, cvtpk(ac[2], ac[3]), tag};
          ST128(o, WS_YP + (((2 * pp + (g >> 1)) * 16 + 4 * w4 + (l15 >> 2)) * 112 + a) * 128 + (2 * (l15 & 3) + (g & 1)) * 16);
        }
        team_bar(tb, 16, lane);
        STAMPW(12, 4);
      } else {
        wg_barrier();
      }
      GET_TAG_SET();
      if (w >= 6) {
        const int h0 = 16 * (w - 6);
        for (u32 spin = 0;; ++spin) {
          const u32 f = __builtin_amdgcn_raw_buffer_load_b32(rs, WS_QF + (a & 15) * 1024 + h0 * 16 + lane * 4, 0, AUX_SC1);
          if (__all(f == tag) || spin > SPIN_CAP) break;
          __builtin_amdgcn_s_sleep(POLLSLEEP);
        }
        __builtin_amdgcn_fence(__ATOMIC_ACQUIRE, "wavefront");
        STAMPW(2, 6);
        for (u32 spin = 0;; ++spin) {
          v4u t[16];
#pragma unroll
          for (int i = 0; i < 16; ++i) t[i] = LD128(WS_QA + (b * 32 + h0 + i) * 1024 + lane * 16);
          bool ok = true;
#pragma unroll
          for (int i = 0; i < 16; ++i) {
            ok &= (t[i].x != SENT) & (t[i].y != SENT) & (t[i].z != SENT) & (t[i].w != SENT);
            *(LAS v4u*)(L + (((u32)(AL_Q + lane * 16) ^ (swz16(i) << 4)) + (u32)((h0 + i) * 1024))) = t[i];
          }
          if (__all(ok) || spin > SPIN_CAP) break;
          __builtin_amdgcn_s_sleep(2);
        }
        STAMPW(3, 6);
      }
      wg_barrier();
      if (w == 5) {
        const v4u sv = {SENT, SENT, SENT, SENT};
#pragma unroll
        for (int i = 0; i < 32; ++i) ST128(sv, WS_PART + (4 * i + g) * 32768 + a * 256 + l15 * 16);
      }
      v4u ft[32];
      {
        const int q = l15 >> 2, pp = l15 & 3, hf2 = g & 1, u2 = g >> 1;
        const u32 r0 = 16 * u2 + 4 * hf2 + q;
        const u32 t0 = (u32)AL_KV + r0 * 1024 + w4 * 256 + ((((u32)pp >> 1) ^ swz16(r0 & 15)) << 4) + 8 * (pp & 1);
        const u32 t1 = (u32)AL_KV + (r0 + 8) * 1024 + w4 * 256 + ((((u32)pp >> 1) ^ swz16((r0 + 8) & 15)) << 4) + 8 * (pp & 1);
#pragma unroll
        for (int ct = 0; ct < 8; ++ct) {
#pragma unroll
          for (int kh = 0; kh < 2; ++kh) {
            const u32 b0 = (t0 ^ (u32)(ct << 5)) + (u32)(kh * 65536), b1 = (t1 ^ (u32)(ct << 5)) + (u32)(kh * 65536);
#pragma unroll
            for (int k2 = 0; k2 < 2; ++k2) {
              const s16x4 x0 = __builtin_amdgcn_ds_read_tr16_b64_v4i16((LAS s16x4*)(L + b0 + k2 * 32768));
              const s16x4 x1 = __builtin_amdgcn_ds_read_tr16_b64_v4i16((LAS s16x4*)(L + b1 + k2 * 32768));
              ft[ct * 4 + 2 * kh + k2] = __builtin_bit_cast(v4u, __builtin_shufflevector(x0, x1, 0, 1, 2, 3, 4, 5, 6, 7));
            }
          }
        }
      }
      STAMPW(13, 4);
      wg_barrier();
      wg_barrier();
      if (w == 5) { drain_vm(); STAMPW(10, 5); }
      wg_barrier();
      v4u pb[2][4];
#pragma unroll
      for (int kt = 0; kt < 4; ++kt)
#pragma unroll
        for (int ht = 0; ht < 2; ++ht) pb[ht][kt] = *(LAS v4u*)(L + AL_P + ((kt * 2 + ht) * 64 + lane) * 16);
#pragma unroll
      for (int T = 0; T < 4; ++T) {
        f32x4 acc[2][2];
#pragma unroll
        for (int c2 = 0; c2 < 2; ++c2)
#pragma unroll
          for (int ht = 0; ht < 2; ++ht) {
            f32x4 z = {0, 0, 0, 0};
#pragma unroll
            for (int kt = 0; kt < 4; ++kt) z = mfma(ft[(2 * T + c2) * 4 + kt], pb[ht][kt], z);
            acc[c2][ht] = z;
          }
#pragma unroll
        for (int ht = 0; ht < 2; ++ht) {
          v4u o;
          o.x = nosent(cvtpk(acc[0][ht][0], acc[0][ht][1])); o.y = nosent(cvtpk(acc[0][ht][2], acc[0][ht][3]));
          o.z = nosent(cvtpk(acc[1][ht][0], acc[1][ht][1])); o.w = nosent(cvtpk(acc[1][ht][2], acc[1][ht][3]));
          *(LAS v4u*)(L + AL_OT + w4 * 8192 + (16 * ht + l15) * 256 + (((u32)(4 * T + g) ^ (u32)l15) << 4)) = o;
        }
      }
#pragma unroll
      for (int i = 0; i < 8; ++i) {
        const u32 hd = 4 * i + g;
        const v4u o = *(LAS v4u*)(L + AL_OT + w4 * 8192 + hd * 256 + (((u32)l15 ^ (hd & 15)) << 4));
        ST128(o, WS_PART + (hd * 4 + w4) * 32768 + a * 256 + l15 * 16);
      }
      STAMPW(7, 4);
    }
    wg_barrier();
    if ((a & 1) == 0) {
      const int fin = a >> 1;
      const int qd = tid & 7;
      const int o_ = fin * 32 + (tid & 31);
      const u32 xraw = *(const GAS u16*)(p.x + o_ * 2);
      v4u vs[2], vy[2];
      vy[1] = v4u{0, tag, 0, tag};
      float r[8];
      for (u32 spin = 0;; ++spin) {
        vs[0] = LD128(WS_OUTP + fin * 16384 + tid * 16);
        vs[1] = LD128(WS_OUTP + fin * 16384 + 8192 + tid * 16);
        vy[0] = LD128(WS_YP + fin * 14336 + tid * 16);
        if (tid < 384) vy[1] = LD128(WS_YP + fin * 14336 + 8192 + tid * 16);
        const bool ok = (vs[0].y == tag) & (vs[0].w == tag) & (vs[1].y == tag) & (vs[1].w == tag) & (vy[0].y == tag) & (vy[0].w == tag) & (vy[1].y == tag) & (vy[1].w == tag);
        r[0] = bf_lo(vs[0].x) + bf_lo(vs[1].x); r[1] = bf_hi(vs[0].x) + bf_hi(vs[1].x); r[2] = bf_lo(vs[0].z) + bf_lo(vs[1].z); r[3] = bf_hi(vs[0].z) + bf_hi(vs[1].z);
        r[4] = bf_lo(vy[0].x) + bf_lo(vy[1].x); r[5] = bf_hi(vy[0].x) + bf_hi(vy[1].x); r[6] = bf_lo(vy[0].z) + bf_lo(vy[1].z); r[7] = bf_hi(vy[0].z) + bf_hi(vy[1].z);
#pragma unroll
        for (int i = 0; i < 8; ++i) { float v = r[i]; v += dpp<0x128>(v); r[i] = xrow16_sum(v); }
        if (__all(ok) || spin > SPIN_CAP) break;
        __builtin_amdgcn_s_sleep(2);
      }
      STAMP(8);
      if (lane < 8) {
        *(LAS f32x4*)(L + AL_FR + (w * 2 + 0) * 128 + qd * 16) = f32x4{r[0], r[1], r[2], r[3]};
        *(LAS f32x4*)(L + AL_FR + (w * 2 + 1) * 128 + qd * 16) = f32x4{r[4], r[5], r[6], r[7]};
      }
      wg_barrier();
      if (tid < 32) {
        float s = 0.f, y = 0.f;
#pragma unroll
        for (int ww = 0; ww < 8; ++ww) {
          s += *(LAS float*)(L + AL_FR + (ww * 2 + 0) * 128 + tid * 4);
          y += *(LAS float*)(L + AL_FR + (ww * 2 + 1) * 128 + tid * 4);
        }
        const float gt = y * __builtin_amdgcn_rcpf(1.f + __expf(-y));
        *(GAS u16*)(p.out + o_ * 2) = (u16)cvtpk(bf_lo(xraw) + s * gt, 0.f);
      }
      if (a == 0 && tid == 0) ST32(tag, WS_EPOCH);
      STAMP(9);
    }
    STAMP_FLUSH();
  }
}

extern "C" void kernel_launch(void* const* d_in, const int* in_sizes, int n_in, void* d_out, int out_size, void* d_ws, size_t ws_size,
                              hipStream_t stream) {
  Params p;
  p.x = (const u8*)d_in[0]; p.ckv = (const u8*)d_in[1]; p.wq = (const u8*)d_in[2]; p.wuk = (const u8*)d_in[3];
  p.wuv = (const u8*)d_in[4]; p.wo = (const u8*)d_in[5]; p.wg = (const u8*)d_in[7]; p.wu = (const u8*)d_in[8]; p.wd = (const u8*)d_in[9];
  p.out = (u8*)d_out; p.ws = (u8*)d_ws;
  static bool attr = false;
  if (!attr) { (void)hipFuncSetAttribute((const void*)mla_ffn_kernel, hipFuncAttributeMaxDynamicSharedMemorySize, LDS_BYTES); attr = true; }
  hipLaunchKernelGGL(mla_ffn_kernel, dim3(NWG), dim3(NTHR), LDS_BYTES, stream, p);
}
```

```cpp
#include <hip/hip_runtime.h>
#include <stdint.h>

typedef unsigned char u8;
typedef unsigned short u16;
typedef unsigned int u32;
typedef unsigned long long u64;
typedef u32 v4u __attribute__((ext_vector_type(4)));
typedef u32 v2u __attribute__((ext_vector_type(2)));
typedef float f32x4 __attribute__((ext_vector_type(4)));
typedef float f32x16 __attribute__((ext_vector_type(16)));
typedef short bf16x8 __attribute__((ext_vector_type(8)));
typedef short s16x4 __attribute__((ext_vector_type(4)));
#define GAS __attribute__((address_space(1)))
#define LAS __attribute__((address_space(3)))
#define DI __device__ __forceinline__

constexpr int NWG = 256, NTHR = 512;
constexpr u32 SENT = 0xFFFFFFFFu;
constexpr int LDS_BYTES = 163840;

constexpr u32 WS_EPOCH = 0;
constexpr u32 WS_QF = 4096;
constexpr u32 WS_QA = 32768;
constexpr u32 WS_QP = WS_QA + 4 * 32768;
constexpr u32 WS_FP = WS_QP + 128 * 2048;
constexpr u32 WS_ST = WS_FP + 112 * 2048;
constexpr u32 WS_OUTP = WS_ST + 128 * 2048;
constexpr u32 WS_YP = WS_OUTP + 64 * 16384;
constexpr u32 WS_PART = WS_YP + 64 * 14336;
constexpr u32 WS_END = WS_PART + 128 * 32768;
constexpr u32 WS_STAMP = 200u << 20;

constexpr int AUX_SC1 = 16;
constexpr u32 SPIN_CAP = 1u << 17;
#define POLLSLEEP 2
#define ADELAY 20
#define CKWIN 8

#define STAMPW(k, wv) do { } while (0)
#define STAMP_FLUSH() do { } while (0)
#define STAMP(k) STAMPW(k, 0)

struct Params {
  const u8* x; const u8* ckv; const u8* wq; const u8* wuk; const u8* wuv; const u8* wo;
  const u8* wg; const u8* wu; const u8* wd; u8* out; u8* ws;
};

DI float bf_lo(u32 v) { return __uint_as_float(v << 16); }
DI float bf_hi(u32 v) { return __uint_as_float(v & 0xFFFF0000u); }
DI u32 cvtpk(float lo, float hi) {
  typedef __bf16 bf2 __attribute__((ext_vector_type(2)));
  typedef float f2 __attribute__((ext_vector_type(2)));
  f2 f = {lo, hi};
  bf2 r = __builtin_convertvector(f, bf2);
  return __builtin_bit_cast(u32, r);
}
DI float rbf(float v) { return bf_lo(cvtpk(v, 0.f)); }
DI u32 nosent(u32 v) { return v < 0xFFFFFFFEu ? v : 0xFFFFFFFEu; }
DI v4u ldnt(const u8* p) { return __builtin_nontemporal_load((const GAS v4u*)p); }
DI f32x4 mfma(v4u a, v4u b, f32x4 c) {
  return __builtin_amdgcn_mfma_f32_16x16x32_bf16(__builtin_bit_cast(bf16x8, a), __builtin_bit_cast(bf16x8, b), c, 0, 0, 0);
}
DI u32 swz16(u32 k) { const u32 k2 = (k >> 2) & 1u, k3 = (k >> 3) & 1u; return k2 | ((k & 3u) << 1) | ((k2 ^ k3) << 3); }
DI void wg_barrier() { asm volatile("s_waitcnt lgkmcnt(0)\n\ts_barrier" ::: "memory"); }
DI void drain_vm() { asm volatile("s_waitcnt vmcnt(0)" ::: "memory"); }
DI float xrow16_max(float x) {
  auto s = __builtin_amdgcn_permlane16_swap(__float_as_uint(x), __float_as_uint(x), false, false);
  x = fmaxf(__uint_as_float(s[0]), __uint_as_float(s[1]));
  auto t = __builtin_amdgcn_permlane32_swap(__float_as_uint(x), __float_as_uint(x), false, false);
  return fmaxf(__uint_as_float(t[0]), __uint_as_float(t[1]));
}
DI float xrow16_sum(float x) {
  auto s = __builtin_amdgcn_permlane16_swap(__float_as_uint(x), __float_as_uint(x), false, false);
  x = __uint_as_float(s[0]) + __uint_as_float(s[1]);
  auto t = __builtin_amdgcn_permlane32_swap(__float_as_uint(x), __float_as_uint(x), false, false);
  return __uint_as_float(t[0]) + __uint_as_float(t[1]);
}
template <int CTRL> DI float dpp(float x) {
  return __builtin_bit_cast(float, __builtin_amdgcn_mov_dpp(__builtin_bit_cast(int, x), CTRL, 0xf, 0xf, true));
}
DI float row16_sum(float v) {
  v += dpp<0xB1>(v); v += dpp<0x4E>(v); v += dpp<0x124>(v); v += dpp<0x128>(v);
  return v;
}
DI float wave_sum(float v) { return xrow16_sum(row16_sum(v)); }
DI void team_bar(LAS u32* cnt, u32 target, int lane) {
  asm volatile("s_waitcnt lgkmcnt(0)" ::: "memory");
  if (lane == 0) __hip_atomic_fetch_add(cnt, 1u, __ATOMIC_RELAXED, __HIP_MEMORY_SCOPE_WORKGROUP);
  while (__hip_atomic_load(cnt, __ATOMIC_RELAXED, __HIP_MEMORY_SCOPE_WORKGROUP) < target) { }
  asm volatile("" ::: "memory");
}
struct Sel { u32 s[2][4]; };
DI Sel make_sel(int l15) {
  Sel r;
  const int jj = l15 & 7;
#pragma unroll
  for (int par = 0; par < 2; ++par) {
    const u32 hb = par ? 0x0302u : 0x0100u;
    const u32 v = (jj & 1) ? ((hb << 16) | 0x0C0Cu) : (0x0C0C0000u | hb);
#pragma unroll
    for (int k = 0; k < 4; ++k) r.s[par][k] = ((jj >> 1) == k) ? v : 0x0C0C0C0Cu;
  }
  return r;
}
DI v4u diagA(const Sel& sel, u32 src, int par) {
  v4u a;
  a.x = __builtin_amdgcn_perm(0u, src, sel.s[par][0]); a.y = __builtin_amdgcn_perm(0u, src, sel.s[par][1]);
  a.z = __builtin_amdgcn_perm(0u, src, sel.s[par][2]); a.w = __builtin_amdgcn_perm(0u, src, sel.s[par][3]);
  return a;
}

#define LD128(off) __builtin_amdgcn_raw_buffer_load_b128(rs, (off), 0, AUX_SC1)
#define LD64(off) __builtin_amdgcn_raw_buffer_load_b64(rs, (off), 0, AUX_SC1)
#define ST128(v, off) __builtin_amdgcn_raw_buffer_store_b128((v), rs, (off), 0, AUX_SC1)
#define ST64(v, off) __builtin_amdgcn_raw_buffer_store_b64((v), rs, (off), 0, AUX_SC1)
#define ST32(v, off) __builtin_amdgcn_raw_buffer_store_b32((v), rs, (off), 0, AUX_SC1)

struct TrB { u32 t0, t1; };
DI TrB tr_base(int lane) {
  const u32 g = lane >> 4, i = lane & 15, q = i >> 2, pp = i & 3;
  const u32 r0 = 8 * g + q, r1 = r0 + 4;
  TrB b;
  b.t0 = r0 * 256 + (((pp >> 1) ^ swz16(r0 & 15)) << 4) + 8 * (pp & 1);
  b.t1 = r1 * 256 + (((pp >> 1) ^ swz16(r1 & 15)) << 4) + 8 * (pp & 1);
  return b;
}
DI bf16x8 tr_frag256(LAS u8* L, const TrB& tb, u32 img_off  , u32 nt) {
  const s16x4 a = __builtin_amdgcn_ds_read_tr16_b64_v4i16((LAS s16x4*)(L + ((tb.t0 ^ (nt << 5)) + img_off)));
  const s16x4 b = __builtin_amdgcn_ds_read_tr16_b64_v4i16((LAS s16x4*)(L + ((tb.t1 ^ (nt << 5)) + img_off)));
  return __builtin_shufflevector(a, b, 0, 1, 2, 3, 4, 5, 6, 7);
}
DI void img256_write(LAS u8* img, int r0, int lane, v4u v) {
  const u32 row = (u32)(r0 + (lane >> 4));
  *(LAS v4u*)(img + row * 256 + (((u32)(lane & 15) ^ swz16(row & 15)) << 4)) = v;
}
DI v4u fr(bf16x8 x) { return __builtin_bit_cast(v4u, x); }
constexpr int WL_XN = 0;
constexpr int WL_Q = 4224;
constexpr int WL_CC = 5376;
constexpr int WL_CX = 6528;
constexpr int WL_IMG = 8192;
static_assert(WL_IMG + 131072 <= LDS_BYTES, "lds");
constexpr int AL_KV = 0;
constexpr int AL_Q = 131072;
constexpr int AL_MX = AL_Q;
constexpr int AL_LS = AL_Q + 1024;
constexpr int AL_P = AL_Q + 2048;
constexpr int AL_XN = 0;
constexpr int AL_RED = 8192;
constexpr int AL_FV = 24576;
constexpr int AL_FH = 26624;
constexpr int AL_FR = 28672;
constexpr int AL_CNT = 31744;
constexpr int AF_XN = AL_Q, AF_RED = AL_Q + 4352, AF_FV = AF_RED + 8192, AF_FH = AF_FV + 2048, AF_CNT = AF_FH + 512;
constexpr int AL_OT = 32768;

__global__ __launch_bounds__(NTHR, 2) void mla_ffn_kernel(Params p) {
  extern __shared__ __attribute__((aligned(16))) u8 smem[];
  LAS u8* L = (LAS u8*)smem;
  const int tid = threadIdx.x, lane = tid & 63;
  const int w = __builtin_amdgcn_readfirstlane(tid >> 6);
  const int bid = blockIdx.x, xcd = bid & 7, rr = bid >> 3;
  const __amdgpu_buffer_rsrc_t rs = __builtin_amdgcn_make_buffer_rsrc((void*)p.ws, 0, (int)WS_END, 0x00020000);
  const int g = lane >> 4, l15 = lane & 15;
  STAMP(0);
  u32 ep_raw = __builtin_amdgcn_raw_buffer_load_b32(rs, WS_EPOCH, 0, 0);
#define GET_TAG() u32 tag; { u32 e_ = ep_raw; asm volatile("" : "+v"(e_)); tag = e_ + 1u; }
#define GET_TAG_DECL() u32 tag
#define GET_TAG_SET() { u32 e_ = ep_raw; asm volatile("" : "+v"(e_)); tag = e_ + 1u; }

#define WARM_LD(off) __builtin_amdgcn_raw_buffer_load_b32(rs, (off), 0, 0)
#define WARM_USE(x) asm volatile("" :: "v"(x))
  u32 wm0 = 0, wm1 = 0, wm2 = 0;
  if (rr < 16) {
    const int h = xcd * 4 + (rr >> 2), j = rr & 3, wi = h * 4 + j;
    const TrB trb = tr_base(lane);
    const int hf_ = lane >> 5;
    v4u xv = {0, 0, 0, 0};
    if (w < 4) xv = *(const GAS v4u*)(p.x + w * 1024 + lane * 16);
    if (w == 4) wm0 = WARM_LD(WS_QP + (wi & ~3) * 2048 + lane * 128);
    if (w == 5) {
      const v4u sv = {SENT, SENT, SENT, SENT};
      ST128(sv, WS_QA + (g * 32 + h) * 1024 + j * 256 + l15 * 16);
      wm0 = WARM_LD(WS_QF + (lane & 15) * 1024 + (wi >> 5) * 128);
    }
    v4u wqv[4];
#pragma unroll
    for (int i = 0; i < 4; ++i) wqv[i] = ldnt(p.wq + ((size_t)(128 * j + 16 * w + 4 * i + g) * 4096 + h * 128) * 2 + l15 * 16);
    __builtin_amdgcn_sched_barrier(0);
    v4u wuk[4];
    if (w < 4) {
      float v[8] = {bf_lo(xv.x), bf_hi(xv.x), bf_lo(xv.y), bf_hi(xv.y), bf_lo(xv.z), bf_hi(xv.z), bf_lo(xv.w), bf_hi(xv.w)};
      float s = 0.f;
#pragma unroll
      for (int i = 0; i < 8; ++i) s += v[i];
      const float mean = wave_sum(s) * (1.f / 512.f);
      float q2 = 0.f;
#pragma unroll
      for (int i = 0; i < 8; ++i) { const float d = v[i] - mean; q2 += d * d; }
      const float mean_b = rbf(mean), den = rbf(sqrtf(rbf(rbf(wave_sum(q2) * (1.f / 512.f)) + 1e-5f)));
#pragma unroll
      for (int i = 0; i < 8; ++i) v[i] = rbf(v[i] - mean_b);
      const float rstd = 1.f / den;
      v4u o;
      o.x = cvtpk(v[0] * rstd, v[1] * rstd); o.y = cvtpk(v[2] * rstd, v[3] * rstd);
      o.z = cvtpk(v[4] * rstd, v[5] * rstd); o.w = cvtpk(v[6] * rstd, v[7] * rstd);
      *(LAS v4u*)(L + WL_XN + w * 1040 + lane * 16) = o;
    }
#pragma unroll
    for (int i = 0; i < 4; ++i)
      wuk[i] = ldnt(p.wuk + ((size_t)(128 * j + 16 * w + 4 * i + g) * 4096 + h * 128) * 2 + l15 * 16);
    __builtin_amdgcn_sched_barrier(0);
#pragma unroll
    for (int i = 0; i < 4; ++i) img256_write(L + WL_IMG, 16 * w + 4 * i, lane, wqv[i]);
    wg_barrier();
    STAMP(1);
    GET_TAG();
    {
      f32x4 acc = {0, 0, 0, 0};
#pragma unroll
      for (int s = 0; s < 4; ++s) {
        const bf16x8 a = tr_frag256(L, trb, WL_IMG + s * 8192, w);
        const v4u b = *(LAS v4u*)(L + WL_XN + (l15 & 3) * 1040 + (128 * j + 32 * s + 8 * g) * 2);
        acc = mfma(fr(a), b, acc);
      }
      if (l15 < 4) {
        const v2u o = {cvtpk(acc[0], acc[1]), cvtpk(acc[2], acc[3])};
        *(LAS v2u*)(L + WL_CC + l15 * 272 + (16 * w + 4 * g) * 2) = o;
      }
      wg_barrier();
      if (w < 2) {
        const u32 rel = (2 * w + (lane >> 5)) * 512 + (lane & 31) * 16;
        {
          const v2u o = *(LAS v2u*)(L + WL_CC + (2 * w + (lane >> 5)) * 272 + (lane & 31) * 8);
          const v4u o4 = {o.x, tag, o.y, tag};
          ST128(o4, WS_QP + wi * 2048 + rel);
        }
        float t[4];
        for (u32 spin = 0;; ++spin) {
          v4u v[4];
#pragma unroll
          for (int q = 0; q < 4; ++q) v[q] = LD128(WS_QP + (h * 4 + q) * 2048 + rel);
          bool ok = true;
          t[0] = t[1] = t[2] = t[3] = 0.f;
#pragma unroll
          for (int q = 0; q < 4; ++q) {
            ok &= (v[q].y == tag) & (v[q].w == tag);
            t[0] += bf_lo(v[q].x); t[1] += bf_hi(v[q].x); t[2] += bf_lo(v[q].z); t[3] += bf_hi(v[q].z);
          }
          if (__all(ok) || spin > SPIN_CAP) break;
          __builtin_amdgcn_s_sleep(1);
        }
        const v2u o2 = {cvtpk(t[0], t[1]), cvtpk(t[2], t[3])};
        *(LAS v2u*)(L + WL_Q + (2 * w + (lane >> 5)) * 272 + (lane & 31) * 8) = o2;
      }
    }
    v4u ukf[4];
    {
      LAS u8* im = L + WL_IMG + 32768 + w * 4096;
#pragma unroll
      for (int i = 0; i < 4; ++i) img256_write(im, 4 * i, lane, wuk[i]);
#pragma unroll
      for (int s = 0; s < 4; ++s) ukf[s] = *(LAS v4u*)(im + l15 * 256 + (((u32)(4 * s + g) ^ swz16(l15)) << 4));
    }
    WARM_USE(wm0);
    if (w == 5) drain_vm();
    wg_barrier();
    STAMP(2);
    {
      f32x4 acc = {0, 0, 0, 0};
#pragma unroll
      for (int s = 0; s < 4; ++s) acc = mfma(ukf[s], *(LAS v4u*)(L + WL_Q + (l15 & 3) * 272 + (32 * s + 8 * g) * 2), acc);
      constexpr float SC = 0.08838834764831845f * 1.4426950408889634f;
      if (l15 < 4) {
        const v2u o = {cvtpk(acc[0] * SC, acc[1] * SC), cvtpk(acc[2] * SC, acc[3] * SC)};
        *(LAS v2u*)(L + WL_CX + l15 * 272 + (16 * w + 4 * g) * 2) = o;
      }
    }
    wg_barrier();
    if (w == 0) {
      v4u o = *(LAS v4u*)(L + WL_CX + g * 272 + l15 * 16);
      o.x = nosent(o.x); o.y = nosent(o.y); o.z = nosent(o.z); o.w = nosent(o.w);
      ST128(o, WS_QA + (g * 32 + h) * 1024 + j * 256 + l15 * 16);
      if (lane < 16) ST32(tag, WS_QF + lane * 1024 + wi * 4);
    }
    STAMP(3);
    if (w == 6) { wm1 = WARM_LD(lane < 16 ? WS_ST + wi * 2048 + lane * 128 : WS_FP + (wi < 112 ? wi : 0) * 2048 + (lane & 15) * 128); }
    if (w == 7) { wm1 = WARM_LD(WS_OUTP + wi * 8192 + lane * 128); wm2 = WARM_LD(WS_YP + wi * 7168 + (lane < 56 ? lane : 0) * 128); }
    bf16x8 fuv[4], fo[4][4];
    {
      v4u t[4];
#pragma unroll
      for (int i = 0; i < 4; ++i)
        t[i] = ldnt(p.wuv + ((size_t)(128 * j + 16 * w + 4 * i + g) * 4096 + h * 128) * 2 + l15 * 16);
      v4u u[4][4];
#define WO_ISSUE(ss) _Pragma("unroll") for (int i = 0; i < 4; ++i) u[ss][i] = ldnt(p.wo + ((size_t)(h * 128 + 32 * (((ss) + j) & 3) + 4 * w + i) * 512) * 2 + lane * 16); __builtin_amdgcn_sched_barrier(0);
      WO_ISSUE(0)
#pragma unroll
      for (int i = 0; i < 4; ++i) img256_write(L + WL_IMG, 16 * w + 4 * i, lane, t[i]);
      wg_barrier();
#pragma unroll
      for (int s = 0; s < 4; ++s) fuv[s] = tr_frag256(L, trb, WL_IMG + s * 8192, w);
#pragma unroll
      for (int ss = 0; ss < 4; ++ss) {
        if (ss == 0) { WO_ISSUE(1) }
        if (ss == 1) { WO_ISSUE(2) }
        if (ss == 2) { WO_ISSUE(3) }
        LAS u8* img = L + WL_IMG + ((ss + 1) & 3) * 32768;
        if (ss == 3) wg_barrier();
#pragma unroll
        for (int i = 0; i < 4; ++i) {
          const u32 row = 4 * w + i;
          *(LAS v4u*)(img + g * 8192 + row * 256 + (((u32)l15 ^ swz16(row & 15)) << 4)) = u[ss][i];
        }
        wg_barrier();
#pragma unroll
        for (int n = 0; n < 4; ++n) fo[ss][n] = tr_frag256(L, trb, WL_IMG + ((ss + 1) & 3) * 32768 + (w >> 1) * 8192, 4 * (w & 1) + n);
      }
    }
    WARM_USE(wm1); WARM_USE(wm2);
    STAMP(4);
    if (w < 4) {
      const int b = w;
      u32 asc[8];
      float invL;
      {
        float m[8], lsum[8];
        for (u32 spin = 0;; ++spin) {
          bool ok = true;
#pragma unroll
          for (int s = 0; s < 8; ++s) {
            const v4u v = LD128(WS_ST + wi * 2048 + (b * 32 + 4 * s + g) * 16);
            ok &= (v.y == tag) & (v.w == tag);
            m[s] = __uint_as_float(v.x); lsum[s] = __uint_as_float(v.z);
          }
          if (__all(ok) || spin > SPIN_CAP) break;
          __builtin_amdgcn_s_sleep(4);
        }
        float M = m[0];
#pragma unroll
        for (int s = 1; s < 8; ++s) M = fmaxf(M, m[s]);
        M = xrow16_max(M);
        float Ls = 0.f;
#pragma unroll
        for (int s = 0; s < 8; ++s) {
          const u32 e = cvtpk(__builtin_amdgcn_exp2f(m[s] - M), 0.f) & 0xFFFFu;
          asc[s] = e;
          Ls += bf_lo(e) * lsum[s];
        }
        Ls = xrow16_sum(Ls);
        invL = __builtin_amdgcn_rcpf(Ls);
      }
      __builtin_amdgcn_fence(__ATOMIC_ACQUIRE, "wavefront");
      STAMP(5);
      v4u v[8];
      for (u32 spin = 0;; ++spin) {
        bool ok = true;
#pragma unroll
        for (int s = 0; s < 8; ++s) {
          v[s] = LD128(WS_PART + wi * 32768 + ((b * 32 + 4 * s + g) * 128) * 2 + l15 * 16);
          ok &= (v[s].x != SENT) & (v[s].y != SENT) & (v[s].z != SENT) & (v[s].w != SENT);
        }
        if (__all(ok) || spin > SPIN_CAP) break;
        __builtin_amdgcn_s_sleep(1);
      }
      STAMP(6);
      f32x4 acc = {0, 0, 0, 0};
#pragma unroll
      for (int s = 0; s < 8; ++s) {
        v4u a = diagA(make_sel(l15), asc[s], 0);
        if (l15 >= 8) a = v4u{0, 0, 0, 0};
        acc = mfma(a, v[s], acc);
      }
      if (g < 2) {
        const v2u o = {cvtpk(acc[0] * invL, acc[1] * invL), cvtpk(acc[2] * invL, acc[3] * invL)};
        *(LAS v2u*)(L + WL_CC + b * 272 + (32 * (l15 >> 2) + 16 * g + 4 * (l15 & 3)) * 2) = o;
      }
    }
    wg_barrier();
    {
      f32x4 acc = {0, 0, 0, 0};
#pragma unroll
      for (int s = 0; s < 4; ++s) acc = mfma(fr(fuv[s]), *(LAS v4u*)(L + WL_CC + (l15 & 3) * 272 + (32 * s + 8 * g) * 2), acc);
      if (l15 < 4) {
        const v2u o = {cvtpk(acc[0], acc[1]), cvtpk(acc[2], acc[3])};
        *(LAS v2u*)(L + WL_CX + l15 * 272 + (16 * w + 4 * g) * 2) = o;
      }
    }
    wg_barrier();
    {
      v4u bb[4];
#pragma unroll
      for (int ss = 0; ss < 4; ++ss) bb[ss] = *(LAS v4u*)(L + WL_CX + (l15 & 3) * 272 + (32 * ((ss + j) & 3) + 8 * g) * 2);
      f32x4 acc[4];
#pragma unroll
      for (int n = 0; n < 4; ++n) {
        acc[n] = f32x4{0, 0, 0, 0};
#pragma unroll
        for (int ss = 0; ss < 4; ++ss) acc[n] = mfma(fr(fo[ss][n]), bb[ss], acc[n]);
      }
      const int u = l15 >> 2;
      const f32x4 a = u == 0 ? acc[0] : (u == 1 ? acc[1] : (u == 2 ? acc[2] : acc[3]));
      const v4u o = {cvtpk(a[0], a[1]), tag, cvtpk(a[2], a[3]), tag};
      ST128(o, WS_OUTP + (((l15 & 3) * 16 + 2 * w + (u >> 1)) * 128 + wi) * 128 + ((u & 1) * 4 + g) * 16);
    }
    STAMP(7);
    STAMP_FLUSH();
  } else {
    const int a = xcd * 16 + (rr - 16), b = a >> 5, ch = a & 31;
    const int fb = a >> 2, ks4 = a & 3;
    const bool ffn = fb < 28;
    v4u xv = {0, 0, 0, 0};
    const int l31 = lane & 31, hf = lane >> 5;
    GET_TAG_DECL();
    if (w < 4) {
      v4u fa[32];
      {
        constexpr int WIN = CKWIN;
        const u8* src = p.ckv + ((size_t)(b * 4096 + ch * 128 + 32 * w) * 512) * 2 + lane * 16;
        __builtin_amdgcn_s_sleep(ADELAY);
        v4u r[WIN];
#pragma unroll
        for (int i = 0; i < WIN; ++i) r[i] = ldnt(src + i * 1024);
        __builtin_amdgcn_sched_barrier(0);
        if (tid == 0) *(LAS u32*)(L + AF_CNT) = 0;
        wg_barrier();
#pragma unroll
        for (int i = 0; i < 32; ++i) {
          const v4u v = r[i % WIN];
          *(LAS v4u*)(L + (((u32)(AL_KV + 32 * w * 1024 + lane * 16) ^ (swz16(i & 15) << 4)) + (u32)(i * 1024))) = v;
          if (i + WIN < 32) r[i % WIN] = ldnt(src + (i + WIN) * 1024);
          __builtin_amdgcn_sched_barrier(0);
        }
        STAMP(1);
      }
      const u32 fsw = ((u32)hf ^ swz16(l15)) << 4;
      {
        const u32 a0 = (u32)(AL_KV + (32 * w + l31) * 1024) + fsw;
#pragma unroll
        for (int s = 0; s < 32; ++s) fa[s] = *(LAS v4u*)(L + ((a0 ^ (u32)((s & 7) << 5)) + (u32)((s >> 3) * 256)));
      }
      wg_barrier();
      STAMP(4);
      f32x16 S, S2;
#pragma unroll
      for (int i = 0; i < 16; ++i) { S[i] = 0.f; S2[i] = 0.f; }
      {
        v4u bq[2][8];
        const u32 q0 = (u32)(AL_Q + l31 * 1024) + fsw;
#define BQ_LOAD(grp) _Pragma("unroll") for (int i = 0; i < 8; ++i) bq[(grp) & 1][i] = *(LAS v4u*)(L + ((q0 ^ (u32)(i << 5)) + (u32)((grp) * 256)));
        BQ_LOAD(0)
#pragma unroll
        for (int gq = 0; gq < 4; ++gq) {
          if (gq < 3) { BQ_LOAD(gq + 1) }
          __builtin_amdgcn_sched_barrier(0);
#pragma unroll
          for (int i = 0; i < 8; i += 2) {
            S = __builtin_amdgcn_mfma_f32_32x32x16_bf16(__builtin_bit_cast(bf16x8, fa[8 * gq + i]), __builtin_bit_cast(bf16x8, bq[gq & 1][i]), S, 0, 0, 0);
            S2 = __builtin_amdgcn_mfma_f32_32x32x16_bf16(__builtin_bit_cast(bf16x8, fa[8 * gq + i + 1]), __builtin_bit_cast(bf16x8, bq[gq & 1][i + 1]), S2, 0, 0, 0);
          }
          __builtin_amdgcn_sched_barrier(0);
        }
      }
      S += S2;
      STAMP(15);
      float mt = S[0];
#pragma unroll
      for (int i = 1; i < 16; ++i) mt = fmaxf(mt, S[i]);
      {
        auto t = __builtin_amdgcn_permlane32_swap(__float_as_uint(mt), __float_as_uint(mt), false, false);
        mt = fmaxf(__uint_as_float(t[0]), __uint_as_float(t[1]));
      }
      STAMP(14);
      wg_barrier();
      if (hf == 0) *(LAS float*)(L + AL_MX + (l31 * 4 + w) * 4) = mt;
      if (tid == 0) *(LAS u32*)(L + AL_CNT) = 0;
      wg_barrier();
      STAMP(5);
      const f32x4 m4 = *(LAS f32x4*)(L + AL_MX + l31 * 16);
      const float M = fmaxf(fmaxf(m4[0], m4[1]), fmaxf(m4[2], m4[3]));
      u32 pk[8];
      float ls = 0.f;
#pragma unroll
      for (int i = 0; i < 8; ++i) {
        pk[i] = cvtpk(__builtin_amdgcn_exp2f(S[2 * i] - M), __builtin_amdgcn_exp2f(S[2 * i + 1] - M));
        ls += bf_lo(pk[i]) + bf_hi(pk[i]);
      }
      {
        auto t = __builtin_amdgcn_permlane32_swap(__float_as_uint(ls), __float_as_uint(ls), false, false);
        ls = __uint_as_float(t[0]) + __uint_as_float(t[1]);
      }
#pragma unroll
      for (int u = 0; u < 2; ++u) {
        const v4u o = {pk[4 * u], pk[4 * u + 1], pk[4 * u + 2], pk[4 * u + 3]};
        *(LAS v4u*)(L + AL_P + (((w * 2 + (l31 >> 4)) * 64) + (2 * u + hf) * 16 + l15) * 16) = o;
      }
      if (hf == 0) *(LAS float*)(L + AL_LS + (l31 * 4 + w) * 4) = ls;
      wg_barrier();
      STAMP(6);
      GET_TAG_SET();
      if (w == 3) {
        const f32x4 l4 = *(LAS f32x4*)(L + AL_LS + l31 * 16);
        const float Ls = (l4[0] + l4[1]) + (l4[2] + l4[3]);
        const v4u o = {__float_as_uint(M), tag, __float_as_uint(Ls), tag};
        ST128(o, WS_ST + (l31 * 4 + hf * 2) * 2048 + a * 16);
        ST128(o, WS_ST + (l31 * 4 + hf * 2 + 1) * 2048 + a * 16);
      }
    } else {
      const int w4 = w - 4;
      if (ffn) {
        LAS u32* tb = (LAS u32*)(L + AF_CNT);
        const Sel sel = make_sel(l15);
        const v4u xv4 = *(const GAS v4u*)(p.x + w4 * 1024 + lane * 16);
        v4u f1[8], f2[4];
        {
          const u8* b1 = (l15 < 8 ? p.wg : p.wu) + ((size_t)(128 * ks4 + 32 * w4 + 8 * g) * 1792 + fb * 64 + (l15 & 7) * 8) * 2;
#pragma unroll
          for (int s = 0; s < 8; ++s) f1[s] = ldnt(b1 + s * 3584);
          __builtin_amdgcn_sched_barrier(0);
        }
        {
          float v[8] = {bf_lo(xv4.x), bf_hi(xv4.x), bf_lo(xv4.y), bf_hi(xv4.y), bf_lo(xv4.z), bf_hi(xv4.z), bf_lo(xv4.w), bf_hi(xv4.w)};
          float s = 0.f;
#pragma unroll
          for (int i = 0; i < 8; ++i) s += v[i];
          const float mean = wave_sum(s) * (1.f / 512.f);
          float q2 = 0.f;
#pragma unroll
          for (int i = 0; i < 8; ++i) { const float d = v[i] - mean; q2 += d * d; }
          const float mean_b = rbf(mean), den = rbf(sqrtf(rbf(rbf(wave_sum(q2) * (1.f / 512.f)) + 1e-5f)));
#pragma unroll
          for (int i = 0; i < 8; ++i) v[i] = rbf(v[i] - mean_b);
          const float rstd = 1.f / den;
          v4u o;
          o.x = cvtpk(v[0] * rstd, v[1] * rstd); o.y = cvtpk(v[2] * rstd, v[3] * rstd);
          o.z = cvtpk(v[4] * rstd, v[5] * rstd); o.w = cvtpk(v[6] * rstd, v[7] * rstd);
          *(LAS v4u*)(L + AF_XN + w4 * 1040 + lane * 16) = o;
        }
        {
          const u8* b2 = p.wd + ((size_t)(fb * 64 + 16 * ks4 + 4 * g) * 512 + 128 * w4 + 8 * l15) * 2;
#pragma unroll
          for (int s = 0; s < 4; ++s) f2[s] = ldnt(b2 + s * 1024);
          __builtin_amdgcn_sched_barrier(0);
        }
        wg_barrier();
        GET_TAG_SET();
#pragma unroll
        for (int pp = 0; pp < 2; ++pp) {
          const v4u xs = *(LAS v4u*)(L + AF_XN + (2 * pp + (l15 >> 3)) * 1040 + (128 * ks4 + 32 * w4 + 8 * g) * 2);
          f32x4 ac = {0, 0, 0, 0};
#pragma unroll
          for (int s = 0; s < 8; ++s) ac = mfma(diagA(sel, xs[s >> 1], s & 1), f1[s], ac);
          *(LAS f32x4*)(L + AF_RED + ((w4 * 2 + pp) * 64 + lane) * 16) = ac;
        }
        team_bar(tb, 4, lane);
        if (w4 < 2) {
          f32x4 s = *(LAS f32x4*)(L + AF_RED + ((0 * 2 + w4) * 64 + lane) * 16);
#pragma unroll
          for (int ww = 1; ww < 4; ++ww) s += *(LAS f32x4*)(L + AF_RED + ((ww * 2 + w4) * 64 + lane) * 16);
          const u32 rel = (2 * w4 + (g >> 1)) * 512 + (8 * l15 + 4 * (g & 1)) * 4;
          const v4u o = {cvtpk(s[0], s[1]), tag, cvtpk(s[2], s[3]), tag};
          ST128(o, WS_FP + a * 2048 + rel);
          const u32 off = WS_FP + (a & ~3) * 2048 + rel;
          float t[4];
          for (u32 spin = 0;; ++spin) {
            v4u v[4];
#pragma unroll
            for (int q = 0; q < 4; ++q) v[q] = LD128(off + q * 2048);
            bool ok = true;
            t[0] = t[1] = t[2] = t[3] = 0.f;
#pragma unroll
            for (int q = 0; q < 4; ++q) {
              ok &= (v[q].y == tag) & (v[q].w == tag);
              t[0] += bf_lo(v[q].x); t[1] += bf_hi(v[q].x); t[2] += bf_lo(v[q].z); t[3] += bf_hi(v[q].z);
            }
            if (__all(ok) || spin > SPIN_CAP) break;
            __builtin_amdgcn_s_sleep(4);
          }
          *(LAS f32x4*)(L + AF_FV + ((2 * w4 + (g >> 1)) * 128 + 8 * l15 + 4 * (g & 1)) * 4) = f32x4{t[0], t[1], t[2], t[3]};
        }
        team_bar(tb, 8, lane);
        STAMPW(11, 4);
        if (w4 == 0) {
          const int bb = g, f = 16 * ks4 + l15;
          const float gv = *(LAS float*)(L + AF_FV + (bb * 128 + f) * 4), uv = *(LAS float*)(L + AF_FV + (bb * 128 + 64 + f) * 4);
          const float gb = rbf(gv);
          const float hv = rbf(gb * __builtin_amdgcn_rcpf(1.f + __expf(-gb))) * rbf(uv);
          *(LAS u16*)(L + AF_FH + bb * 64 + l15 * 2) = (u16)cvtpk(hv, 0.f);
        }
        team_bar(tb, 12, lane);
#pragma unroll
        for (int pp = 0; pp < 2; ++pp) {
          const v2u hs = *(LAS v2u*)(L + AF_FH + (2 * pp + (l15 >> 3)) * 64 + (4 * g) * 2);
          f32x4 ac = {0, 0, 0, 0};
#pragma unroll
          for (int s = 0; s < 4; ++s) ac = mfma(diagA(sel, hs[s >> 1], s & 1), f2[s], ac);
          const v4u o = {cvtpk(ac[0], ac[1]), tag, cvtpk(ac[2], ac[3]), tag};
          ST128(o, WS_YP + (((2 * pp + (g >> 1)) * 16 + 4 * w4 + (l15 >> 2)) * 112 + a) * 128 + (2 * (l15 & 3) + (g & 1)) * 16);
        }
        team_bar(tb, 16, lane);
        STAMPW(12, 4);
      } else {
        wg_barrier();
      }
      GET_TAG_SET();
      if (w >= 6) {
        const int h0 = 16 * (w - 6);
        for (u32 spin = 0;; ++spin) {
          const u32 f = __builtin_amdgcn_raw_buffer_load_b32(rs, WS_QF + (a & 15) * 1024 + h0 * 16 + lane * 4, 0, AUX_SC1);
          if (__all(f == tag) || spin > SPIN_CAP) break;
          __builtin_amdgcn_s_sleep(POLLSLEEP);
        }
        __builtin_amdgcn_fence(__ATOMIC_ACQUIRE, "wavefront");
        STAMPW(2, 6);
        for (u32 spin = 0;; ++spin) {
          v4u t[16];
#pragma unroll
          for (int i = 0; i < 16; ++i) t[i] = LD128(WS_QA + (b * 32 + h0 + i) * 1024 + lane * 16);
          bool ok = true;
#pragma unroll
          for (int i = 0; i < 16; ++i) {
            ok &= (t[i].x != SENT) & (t[i].y != SENT) & (t[i].z != SENT) & (t[i].w != SENT);
            *(LAS v4u*)(L + (((u32)(AL_Q + lane * 16) ^ (swz16(i) << 4)) + (u32)((h0 + i) * 1024))) = t[i];
          }
          if (__all(ok) || spin > SPIN_CAP) break;
          __builtin_amdgcn_s_sleep(2);
        }
        STAMPW(3, 6);
      }
      wg_barrier();
      if (w == 5) {
        const v4u sv = {SENT, SENT, SENT, SENT};
#pragma unroll
        for (int i = 0; i < 32; ++i) ST128(sv, WS_PART + (4 * i + g) * 32768 + a * 256 + l15 * 16);
      }
      v4u ft[32];
      {
        const int q = l15 >> 2, pp = l15 & 3, hf2 = g & 1, u2 = g >> 1;
        const u32 r0 = 16 * u2 + 4 * hf2 + q;
        const u32 t0 = (u32)AL_KV + r0 * 1024 + w4 * 256 + ((((u32)pp >> 1) ^ swz16(r0 & 15)) << 4) + 8 * (pp & 1);
        const u32 t1 = (u32)AL_KV + (r0 + 8) * 1024 + w4 * 256 + ((((u32)pp >> 1) ^ swz16((r0 + 8) & 15)) << 4) + 8 * (pp & 1);
#pragma unroll
        for (int ct = 0; ct < 8; ++ct) {
#pragma unroll
          for (int kh = 0; kh < 2; ++kh) {
            const u32 b0 = (t0 ^ (u32)(ct << 5)) + (u32)(kh * 65536), b1 = (t1 ^ (u32)(ct << 5)) + (u32)(kh * 65536);
#pragma unroll
            for (int k2 = 0; k2 < 2; ++k2) {
              const s16x4 x0 = __builtin_amdgcn_ds_read_tr16_b64_v4i16((LAS s16x4*)(L + b0 + k2 * 32768));
              const s16x4 x1 = __builtin_amdgcn_ds_read_tr16_b64_v4i16((LAS s16x4*)(L + b1 + k2 * 32768));
              ft[ct * 4 + 2 * kh + k2] = __builtin_bit_cast(v4u, __builtin_shufflevector(x0, x1, 0, 1, 2, 3, 4, 5, 6, 7));
            }
          }
        }
      }
      STAMPW(13, 4);
      wg_barrier();
      wg_barrier();
      if (w == 5) { drain_vm(); STAMPW(10, 5); }
      wg_barrier();
      v4u pb[2][4];
#pragma unroll
      for (int kt = 0; kt < 4; ++kt)
#pragma unroll
        for (int ht = 0; ht < 2; ++ht) pb[ht][kt] = *(LAS v4u*)(L + AL_P + ((kt * 2 + ht) * 64 + lane) * 16);
#pragma unroll
      for (int T = 0; T < 4; ++T) {
        f32x4 acc[2][2];
#pragma unroll
        for (int c2 = 0; c2 < 2; ++c2)
#pragma unroll
          for (int ht = 0; ht < 2; ++ht) {
            f32x4 z = {0, 0, 0, 0};
#pragma unroll
            for (int kt = 0; kt < 4; ++kt) z = mfma(ft[(2 * T + c2) * 4 + kt], pb[ht][kt], z);
            acc[c2][ht] = z;
          }
#pragma unroll
        for (int ht = 0; ht < 2; ++ht) {
          v4u o;
          o.x = nosent(cvtpk(acc[0][ht][0], acc[0][ht][1])); o.y = nosent(cvtpk(acc[0][ht][2], acc[0][ht][3]));
          o.z = nosent(cvtpk(acc[1][ht][0], acc[1][ht][1])); o.w = nosent(cvtpk(acc[1][ht][2], acc[1][ht][3]));
          *(LAS v4u*)(L + AL_OT + w4 * 8192 + (16 * ht + l15) * 256 + (((u32)(4 * T + g) ^ (u32)l15) << 4)) = o;
        }
      }
#pragma unroll
      for (int i = 0; i < 8; ++i) {
        const u32 hd = 4 * i + g;
        const v4u o = *(LAS v4u*)(L + AL_OT + w4 * 8192 + hd * 256 + (((u32)l15 ^ (hd & 15)) << 4));
        ST128(o, WS_PART + (hd * 4 + w4) * 32768 + a * 256 + l15 * 16);
      }
      STAMPW(7, 4);
    }
    wg_barrier();
    if ((a & 1) == 0) {
      const int fin = a >> 1;
      const int qd = tid & 7;
      const int o_ = fin * 32 + (tid & 31);
      const u32 xraw = *(const GAS u16*)(p.x + o_ * 2);
      v4u vs[2], vy[2];
      vy[1] = v4u{0, tag, 0, tag};
      float r[8];
      for (u32 spin = 0;; ++spin) {
        vs[0] = LD128(WS_OUTP + fin * 16384 + tid * 16);
        vs[1] = LD128(WS_OUTP + fin * 16384 + 8192 + tid * 16);
        vy[0] = LD128(WS_YP + fin * 14336 + tid * 16);
        if (tid < 384) vy[1] = LD128(WS_YP + fin * 14336 + 8192 + tid * 16);
        const bool ok = (vs[0].y == tag) & (vs[0].w == tag) & (vs[1].y == tag) & (vs[1].w == tag) & (vy[0].y == tag) & (vy[0].w == tag) & (vy[1].y == tag) & (vy[1].w == tag);
        r[0] = bf_lo(vs[0].x) + bf_lo(vs[1].x); r[1] = bf_hi(vs[0].x) + bf_hi(vs[1].x); r[2] = bf_lo(vs[0].z) + bf_lo(vs[1].z); r[3] = bf_hi(vs[0].z) + bf_hi(vs[1].z);
        r[4] = bf_lo(vy[0].x) + bf_lo(vy[1].x); r[5] = bf_hi(vy[0].x) + bf_hi(vy[1].x); r[6] = bf_lo(vy[0].z) + bf_lo(vy[1].z); r[7] = bf_hi(vy[0].z) + bf_hi(vy[1].z);
#pragma unroll
        for (int i = 0; i < 8; ++i) { float v = r[i]; v += dpp<0x128>(v); r[i] = xrow16_sum(v); }
        if (__all(ok) || spin > SPIN_CAP) break;
        __builtin_amdgcn_s_sleep(2);
      }
      STAMP(8);
      if (lane < 8) {
        *(LAS f32x4*)(L + AL_FR + (w * 2 + 0) * 128 + qd * 16) = f32x4{r[0], r[1], r[2], r[3]};
        *(LAS f32x4*)(L + AL_FR + (w * 2 + 1) * 128 + qd * 16) = f32x4{r[4], r[5], r[6], r[7]};
      }
      wg_barrier();
      if (tid < 32) {
        float s = 0.f, y = 0.f;
#pragma unroll
        for (int ww = 0; ww < 8; ++ww) {
          s += *(LAS float*)(L + AL_FR + (ww * 2 + 0) * 128 + tid * 4);
          y += *(LAS float*)(L + AL_FR + (ww * 2 + 1) * 128 + tid * 4);
        }
        const float yb = rbf(y);
        const float gt = rbf(yb * __builtin_amdgcn_rcpf(1.f + __expf(-yb)));
        *(GAS u16*)(p.out + o_ * 2) = (u16)cvtpk(bf_lo(xraw) + rbf(rbf(s) * gt), 0.f);
      }
      if (a == 0 && tid == 0) ST32(tag, WS_EPOCH);
      STAMP(9);
    }
    STAMP_FLUSH();
  }
}

extern "C" void kernel_launch(void* const* d_in, const int* in_sizes, int n_in, void* d_out, int out_size, void* d_ws, size_t ws_size,
                              hipStream_t stream) {
  Params p;
  p.x = (const u8*)d_in[0]; p.ckv = (const u8*)d_in[1]; p.wq = (const u8*)d_in[2]; p.wuk = (const u8*)d_in[3];
  p.wuv = (const u8*)d_in[4]; p.wo = (const u8*)d_in[5]; p.wg = (const u8*)d_in[7]; p.wu = (const u8*)d_in[8]; p.wd = (const u8*)d_in[9];
  p.out = (u8*)d_out; p.ws = (u8*)d_ws;
  (void)hipFuncSetAttribute((const void*)mla_ffn_kernel, hipFuncAttributeMaxDynamicSharedMemorySize, LDS_BYTES);
  hipLaunchKernelGGL(mla_ffn_kernel, dim3(NWG), dim3(NTHR), LDS_BYTES, stream, p);
}
```
